```python
import torch

def _softmax(x, axis=-1):
    return torch.softmax(x, dim=axis)
def _gelu(x):
    return torch.nn.functional.gelu(x, approximate="tanh")
def _silu(x):
    return torch.nn.functional.silu(x)
def _sigmoid(x):
    return torch.sigmoid(x)
def _layernorm(x, axis=-1, eps=1e-5):
    m = x.mean(dim=axis, keepdim=True)
    v = x.var(dim=axis, keepdim=True, unbiased=False)
    return (x - m) / torch.sqrt(v + eps)
def _scatter_add(tgt, idx, src, axis=0):
    return tgt.clone().index_add_(axis, idx, src)
def _segment_sum(src, seg, n):
    out = torch.zeros((n,) + src.shape[1:], dtype=src.dtype)
    return out.index_add_(0, seg, src)
def _pad(x, pw):
    flat = tuple(v for pair in reversed(pw) for v in pair)
    return torch.nn.functional.pad(x, flat)

kernel_name = 'parallel_sum__gqa_block_cached__geglu_ffn__decode'

def setup_inputs():

    _w = (lambda a: (lambda t: t.to(torch.bfloat16) if t.is_floating_point() else t)(torch.as_tensor(a)))
    def _A_setup():
        B, Sq, D = 1, 1, 1024
        Hq, Hkv, Sk, Dh = 32, 4, 65536, 128
        assert Hq % Hkv == 0, (Hq, Hkv)
        return {
            "x": _w((lambda *s: torch.randn(*s, dtype=torch.bfloat16) * 0.05)(B, Sq, D)),
            "k_cache": _w((lambda *s: torch.randn(*s, dtype=torch.bfloat16) * 3.2)(B, Hkv, Sk, Dh)),
            "v_cache": _w((lambda *s: torch.randn(*s, dtype=torch.bfloat16) * 1.6)(B, Hkv, Sk, Dh)),
            "wq": _w((lambda *s: torch.randn(*s, dtype=torch.bfloat16) * 3.2)(D, Hq * Dh) * (D ** -0.5)),
            "wo": _w((lambda *s: torch.randn(*s, dtype=torch.bfloat16) * 1.6)(Hq * Dh, D) * ((Hq * Dh) ** -0.5)),
        }
    _A_in = _A_setup()

    def _B_setup():
        B, S, D, F = 1, 1, 1024, 3584
        return {
            "x": _w((lambda *s: torch.randn(*s, dtype=torch.bfloat16) * 0.05)(B, S, D)),
            "w_gate": _w((lambda *s: torch.randn(*s, dtype=torch.bfloat16) * 0.05)(D, F)),
            "w_up": _w((lambda *s: torch.randn(*s, dtype=torch.bfloat16) * 0.05)(D, F)),
            "w_down": _w((lambda *s: torch.randn(*s, dtype=torch.bfloat16) * 0.05)(F, D)),
        }
    _B_in = _B_setup()

    return {
        "A_x": _A_in["x"],
        "A_k_cache": _A_in["k_cache"],
        "A_v_cache": _A_in["v_cache"],
        "A_wq": _A_in["wq"],
        "A_wo": _A_in["wo"],
        "B_x": _B_in["x"],
        "B_w_gate": _B_in["w_gate"],
        "B_w_up": _B_in["w_up"],
        "B_w_down": _B_in["w_down"],
    }

def reference(A_x, A_k_cache, A_v_cache, A_wq, A_wo, B_x, B_w_gate, B_w_up, B_w_down):
    def _A_ref(x, k_cache, v_cache, wq, wo):
        B, Sq, D = x.shape
        Hq, Dh = 32, 128
        Hkv = k_cache.shape[1]
        rep = Hq // Hkv
        q = (lambda x, *d: x.permute(*d))((x @ wq).reshape(B, Sq, Hq, Dh), 0, 2, 1, 3)
        k = (lambda x, n, axis: torch.repeat_interleave(x, n, dim=axis))(k_cache, rep, axis=1)
        v = (lambda x, n, axis: torch.repeat_interleave(x, n, dim=axis))(v_cache, rep, axis=1)
        scores = torch.einsum("bhqd,bhkd->bhqk", q, k) * (Dh ** -0.5)
        p = _softmax(scores, axis=-1)
        o = torch.einsum("bhqk,bhkd->bhqd", p, v)
        return (lambda x, *d: x.permute(*d))(o, 0, 2, 1, 3).reshape(B, Sq, Hq * Dh) @ wo

    def _B_ref(x, w_gate, w_up, w_down):
        return (_gelu(x @ w_gate) * (x @ w_up)) @ w_down

    x = A_x
    xn = _layernorm(x, axis=-1)
    return x + _A_ref(xn, A_k_cache, A_v_cache, A_wq, A_wo) + _B_ref(xn, B_w_gate, B_w_up, B_w_down)
```

```cpp
#include <hip/hip_runtime.h>
#include <hip/hip_bf16.h>
#include <stdint.h>

#define MIMIC 0

typedef unsigned u32x4 __attribute__((ext_vector_type(4)));
typedef float f32x4 __attribute__((ext_vector_type(4)));
typedef short bf16x8 __attribute__((ext_vector_type(8)));

constexpr int D = 1024, HQ = 32, HKV = 4, SK = 65536, DH = 128, F = 3584;
constexpr int NWG = 256;

__device__ __forceinline__ float bf2f(unsigned short b) { return __builtin_bit_cast(float, (unsigned)b << 16); }
__device__ __forceinline__ float bflo(unsigned u) { return __builtin_bit_cast(float, u << 16); }
__device__ __forceinline__ float bfhi(unsigned u) { return __builtin_bit_cast(float, u & 0xffff0000u); }
__device__ __forceinline__ unsigned short f2bf(float f) { return __builtin_bit_cast(unsigned short, (__bf16)f); }
__device__ __forceinline__ float rbf(float f) { return (float)(__bf16)f; }
__device__ __forceinline__ f32x4 mfma(u32x4 a, u32x4 b, f32x4 c) {
  return __builtin_amdgcn_mfma_f32_16x16x32_bf16(__builtin_bit_cast(bf16x8, a), __builtin_bit_cast(bf16x8, b), c, 0, 0, 0);
}
template <int CTRL> __device__ __forceinline__ float dpp(float x) {
  return __builtin_bit_cast(float, __builtin_amdgcn_mov_dpp(__builtin_bit_cast(int, x), CTRL, 0xf, 0xf, true));
}
__device__ __forceinline__ float wave_sum(float v) {
#pragma unroll
  for (int o = 32; o > 0; o >>= 1) v += __shfl_xor(v, o);
  return v;
}

__device__ __forceinline__ void layernorm_lds(const unsigned short* x, float* xn, float* red, int tid, int nthr) {
  float s = 0.f;
  for (int i = tid; i < D; i += nthr) s += bf2f(x[i]);
  s = wave_sum(s);
  if ((tid & 63) == 0) red[tid >> 6] = s;
  __syncthreads();
  float tot = 0.f;
  for (int w = 0; w < nthr / 64; ++w) tot += red[w];
  const float mean = tot * (1.f / D);
  __syncthreads();
  float v = 0.f;
  for (int i = tid; i < D; i += nthr) { float d = bf2f(x[i]) - mean; v += d * d; }
  v = wave_sum(v);
  if ((tid & 63) == 0) red[tid >> 6] = v;
  __syncthreads();
  float vt = 0.f;
  for (int w = 0; w < nthr / 64; ++w) vt += red[w];
  const float var = vt * (1.f / D);
  const float rs = 1.f / sqrtf(var + 1e-5f);
  for (int i = tid; i < D; i += nthr) xn[i] = (bf2f(x[i]) - mean) * rs;
  __syncthreads();
}

__device__ __forceinline__ float gelu_tanh(float g) {
  const float u = 0.7978845608028654f * (g + 0.044715f * g * g * g);
  return 0.5f * g * (1.f + tanhf(u));
}

__global__ void __launch_bounds__(256) k_proj(const unsigned short* x, const unsigned short* wq, const unsigned short* wg,
                                              const unsigned short* wu, unsigned short* qout, float* hout) {
  __shared__ float xn[D];
  __shared__ float red[8];
  __shared__ float part[2][4][16];
  const int tid = threadIdx.x, b = blockIdx.x;
  layernorm_lds(x, xn, red, tid, 256);
  const int half = tid & 1, rg = tid >> 1;
  const bool isq = b < 256;
  const int nmat = isq ? 1 : 2;
  for (int m = 0; m < nmat; ++m) {
    const unsigned short* W = isq ? wq : (m == 0 ? wg : wu);
    const int ld = isq ? 4096 : F;
    const int c0 = (isq ? b : b - 256) * 16 + half * 8;
    float acc[8];
#pragma unroll
    for (int j = 0; j < 8; ++j) acc[j] = 0.f;
#pragma unroll
    for (int r = 0; r < 8; ++r) {
      const int row = r * 128 + rg;
      const u32x4 w = *(const u32x4*)(W + (size_t)row * ld + c0);
      const float xv = xn[row];
#pragma unroll
      for (int j = 0; j < 4; ++j) { acc[2 * j] += xv * bflo(w[j]); acc[2 * j + 1] += xv * bfhi(w[j]); }
    }
#pragma unroll
    for (int j = 0; j < 8; ++j) {
      float v = acc[j];
#pragma unroll
      for (int o = 32; o > 1; o >>= 1) v += __shfl_xor(v, o);
      acc[j] = v;
    }
    if ((tid & 63) < 2) {
#pragma unroll
      for (int j = 0; j < 8; ++j) part[m][tid >> 6][half * 8 + j] = acc[j];
    }
  }
  __syncthreads();
  if (tid < 16) {
    const float a0 = part[0][0][tid] + part[0][1][tid] + part[0][2][tid] + part[0][3][tid];
    if (isq) qout[b * 16 + tid] = f2bf(a0);
    else {
      const float a1 = part[1][0][tid] + part[1][1][tid] + part[1][2][tid] + part[1][3][tid];
      hout[(b - 256) * 16 + tid] = gelu_tanh(a0) * a1;
    }
  }
}

__global__ void __launch_bounds__(512) k_attn(const unsigned short* q, const unsigned short* K, const unsigned short* V, float* pm, float* pl, float* po) {
  __shared__ float sm[8][8], sl[8][8];
  __shared__ float so[8][8][128];
  const int tid = threadIdx.x, l = tid & 63, w = tid >> 6, b = blockIdx.x;
  const int kvh = b >> 6, split = b & 63;
  const int g = l >> 4, head = l & 7, c2 = (l >> 3) & 1;
  const int key0 = split * 1024 + w * 128;
  const unsigned short* Kb = K + ((size_t)kvh * SK + key0) * DH;
  const unsigned short* Vb = V + ((size_t)kvh * SK + key0) * DH;
  u32x4 qf[2];
#pragma unroll
  for (int s = 0; s < 2; ++s) qf[s] = *(const u32x4*)(q + (kvh * 8 + head) * DH + (8 * s + 2 * g + c2) * 8);
  float sc[16];
  const int krow = (l & 15) >> 1, c1 = l & 1;
#pragma unroll
  for (int grp = 0; grp < 16; ++grp) {
    f32x4 d = {0.f, 0.f, 0.f, 0.f};
#pragma unroll
    for (int s = 0; s < 2; ++s) {
      const u32x4 kf = __builtin_nontemporal_load((const u32x4*)(Kb + (size_t)(8 * grp + krow) * DH + (8 * s + 2 * g + c1) * 8));
      d = mfma(kf, qf[s], d);
    }
    const float keep = c2 ? d[3] : d[0], send = c2 ? d[1] : d[2];
    float v = keep + dpp<0x128>(send);
    v *= 0.08838834764831845f * 1.4426950408889634f;
    sc[grp] = v;
  }
  float m = sc[0];
#pragma unroll
  for (int i = 1; i < 16; ++i) m = fmaxf(m, sc[i]);
  m = fmaxf(m, __shfl_xor(m, 8)); m = fmaxf(m, __shfl_xor(m, 16)); m = fmaxf(m, __shfl_xor(m, 32));
  float lsum = 0.f;
  f32x4 acc[8];
#pragma unroll
  for (int j = 0; j < 8; ++j) acc[j] = f32x4{0.f, 0.f, 0.f, 0.f};
#pragma unroll
  for (int blk = 0; blk < 2; ++blk) {
    u32x4 pa;
#pragma unroll
    for (int u = 0; u < 4; ++u) {
      const float p0 = __builtin_amdgcn_exp2f(sc[blk * 8 + 2 * u] - m), p1 = __builtin_amdgcn_exp2f(sc[blk * 8 + 2 * u + 1] - m);
      const unsigned short b0 = f2bf(p0), b1 = f2bf(p1);
      lsum += bf2f(b0) + bf2f(b1);
      pa[u] = (unsigned)b0 | ((unsigned)b1 << 16);
    }
#pragma unroll
    for (int ks = 0; ks < 2; ++ks) {
      u32x4 a;
#pragma unroll
      for (int u = 0; u < 4; ++u) a[u] = (c2 == ks) ? pa[u] : 0u;
      u32x4 vr[8];
#pragma unroll
      for (int jp = 0; jp < 8; ++jp)
        vr[jp] = __builtin_nontemporal_load((const u32x4*)(Vb + (size_t)(64 * blk + 8 * jp + 2 * g + ks) * DH + (l & 15) * 8));
#pragma unroll
      for (int j = 0; j < 8; ++j) {
        u32x4 f;
#pragma unroll
        for (int u = 0; u < 4; ++u) f[u] = __builtin_amdgcn_perm(vr[2 * u + 1][j >> 1], vr[2 * u][j >> 1], (j & 1) ? 0x07060302u : 0x05040100u);
        acc[j] = mfma(a, f, acc[j]);
      }
    }
  }
  lsum += __shfl_xor(lsum, 8); lsum += __shfl_xor(lsum, 16); lsum += __shfl_xor(lsum, 32);
  if (l < 8) { sm[w][l] = m; sl[w][l] = lsum; }
#pragma unroll
  for (int j = 0; j < 8; ++j)
#pragma unroll
    for (int r = 0; r < 4; ++r) {
      float v = acc[j][r] + __shfl_xor(acc[j][r], 32);
      if (l < 32) so[w][4 * (l >> 4) + r][8 * (l & 15) + j] = v;
    }
  __syncthreads();
  {
    const int hh = tid >> 6, d0 = 2 * (tid & 63);
    float M = sm[0][hh];
#pragma unroll
    for (int ww = 1; ww < 8; ++ww) M = fmaxf(M, sm[ww][hh]);
    float L = 0.f, o0 = 0.f, o1 = 0.f;
#pragma unroll
    for (int ww = 0; ww < 8; ++ww) {
      const float wt = __builtin_amdgcn_exp2f(sm[ww][hh] - M);
      L += wt * sl[ww][hh]; o0 += wt * so[ww][hh][d0]; o1 += wt * so[ww][hh][d0 + 1];
    }
    const int hq = kvh * 8 + hh;
    po[((size_t)hq * 64 + split) * DH + d0] = o0;
    po[((size_t)hq * 64 + split) * DH + d0 + 1] = o1;
    if ((tid & 63) == 0) { pm[hq * 64 + split] = M; pl[hq * 64 + split] = L; }
  }
}

__global__ void __launch_bounds__(256) k_out(const float* pm, const float* pl, const float* po, const float* h, const unsigned short* wo,
                                             const unsigned short* wd, float* part) {
  __shared__ float so[16], sw[64];
  const int tid = threadIdx.x, j = blockIdx.x, hq = j >> 3, d0 = (j & 7) * 16;
  if (tid < 64) {
    const float mm = pm[hq * 64 + tid];
    float M = mm;
#pragma unroll
    for (int o = 32; o > 0; o >>= 1) M = fmaxf(M, __shfl_xor(M, o));
    const float wt = __builtin_amdgcn_exp2f(mm - M);
    const float L = wave_sum(wt * pl[hq * 64 + tid]);
    sw[tid] = wt / L;
  }
  __syncthreads();
  if (tid < 16) {
    float o = 0.f;
    for (int s = 0; s < 64; ++s) o += sw[s] * po[((size_t)hq * 64 + s) * DH + d0 + tid];
    so[tid] = o;
  }
  __syncthreads();
  float acc[4] = {0.f, 0.f, 0.f, 0.f};
  for (int r = 0; r < 16; ++r) {
    const uint2 wv = *(const uint2*)(wo + (size_t)(16 * j + r) * D + tid * 4);
    const float o = so[r];
    acc[0] += o * bflo(wv.x); acc[1] += o * bfhi(wv.x); acc[2] += o * bflo(wv.y); acc[3] += o * bfhi(wv.y);
  }
  float acc2[4] = {0.f, 0.f, 0.f, 0.f};
  for (int r = 0; r < 14; ++r) {
    const uint2 wv = *(const uint2*)(wd + (size_t)(14 * j + r) * D + tid * 4);
    const float o = h[14 * j + r];
    acc2[0] += o * bflo(wv.x); acc2[1] += o * bfhi(wv.x); acc2[2] += o * bflo(wv.y); acc2[3] += o * bfhi(wv.y);
  }
#pragma unroll
  for (int i = 0; i < 4; ++i) { part[(size_t)j * D + tid * 4 + i] = acc[i]; part[(size_t)(256 + j) * D + tid * 4 + i] = acc2[i]; }
}

__global__ void __launch_bounds__(256) k_final(const unsigned short* x, const float* part, unsigned short* out) {
  __shared__ float red[2][16][16];
  const int tid = threadIdx.x, col = blockIdx.x * 16 + (tid & 15), rg = tid >> 4;
  float a = 0.f, bsum = 0.f;
  for (int r = rg; r < 256; r += 16) { a += part[(size_t)r * D + col]; bsum += part[(size_t)(256 + r) * D + col]; }
  red[0][rg][tid & 15] = a; red[1][rg][tid & 15] = bsum;
  __syncthreads();
  if (tid < 16) {
    float A = 0.f, B = 0.f;
    for (int r = 0; r < 16; ++r) { A += red[0][r][tid]; B += red[1][r][tid]; }
    out[col] = f2bf(bf2f(x[col]) + A + B);
  }
}

extern "C" void kernel_launch(void* const* d_in, const int* in_sizes, int n_in, void* d_out, int out_size, void* d_ws, size_t ws_size,
                              hipStream_t stream) {
  const unsigned short* x = (const unsigned short*)d_in[0];
  const unsigned short* K = (const unsigned short*)d_in[1];
  const unsigned short* V = (const unsigned short*)d_in[2];
  const unsigned short* wq = (const unsigned short*)d_in[3];
  const unsigned short* wo = (const unsigned short*)d_in[4];
  const unsigned short* wg = (const unsigned short*)d_in[6];
  const unsigned short* wu = (const unsigned short*)d_in[7];
  const unsigned short* wd = (const unsigned short*)d_in[8];
  char* ws = (char*)d_ws;
  unsigned short* q = (unsigned short*)ws;
  float* h = (float*)(ws + 16384);
  float* pm = (float*)(ws + 65536);
  float* pl = (float*)(ws + 65536 + 8192);
  float* po = (float*)(ws + 131072);
  float* part = (float*)(ws + 131072 + 1048576);
  k_proj<<<256 + 224, 256, 0, stream>>>(x, wq, wg, wu, q, h);
  k_attn<<<256, 512, 0, stream>>>(q, K, V, pm, pl, po);
  k_out<<<256, 256, 0, stream>>>(pm, pl, po, h, wo, wd, part);
  k_final<<<64, 256, 0, stream>>>(x, part, (unsigned short*)d_out);
}
```

```cpp
#include <hip/hip_runtime.h>
#include <hip/hip_bf16.h>
#include <stdint.h>

#define DYN 0

typedef unsigned u32;
typedef unsigned long long u64;
typedef unsigned short u16;
typedef u32 u32x4 __attribute__((ext_vector_type(4)));
typedef u32 u32x2 __attribute__((ext_vector_type(2)));
typedef float f32x4 __attribute__((ext_vector_type(4)));
typedef short bf16x8 __attribute__((ext_vector_type(8)));
typedef short bf16x4 __attribute__((ext_vector_type(4)));
#define GAS __attribute__((address_space(1)))
typedef GAS u64 gu64;
typedef GAS u32 gu32;
#define RLX_AGENT __ATOMIC_RELAXED, __HIP_MEMORY_SCOPE_AGENT

constexpr int DM = 1024, SK = 65536, DH = 128, FF = 3584;
constexpr int NWG = 256, NT = 256;
constexpr int PSTAT = 4;
constexpr int NUNIT_POOL = 1024;
constexpr u32 WS_EPOCH = 0;
constexpr u32 WS_CNT = 4096;
constexpr u32 WS_Q = 16384;
constexpr u32 WS_GU = WS_Q + 4 * 1024 * 8;
constexpr u32 WS_LSE = WS_GU + 2 * 4 * FF * 8;
constexpr u32 WS_O = WS_LSE + 4 * 8 * 64 * 16;
constexpr u32 WS_IN = WS_O + 4 * 8 * 2 * 64 * 64 * 8;
constexpr u32 WS_END = WS_IN + 256 * 64 * 4 * 8;
constexpr u32 WS_DBG = WS_END;
constexpr u32 SPIN_CAP = 1u << 17;
constexpr int L_XN = 0;
constexpr int L_QP = L_XN + 4096;
constexpr int L_QS = L_QP + 1024;
constexpr int L_SM = L_QS + 2048;
constexpr int L_SL = L_SM + 128;
constexpr int L_H = L_SL + 128;
constexpr int L_OP = L_H + 256;
constexpr int L_SO = L_OP + 1024;
constexpr int L_BYTES = 88 * 1024;

struct Params {
  const u16* x; const u16* K; const u16* V; const u16* wq; const u16* wo; const u16* wg; const u16* wu; const u16* wd;
  u16* out; unsigned char* ws;
};

__device__ __forceinline__ float bflo(u32 u) { return __builtin_bit_cast(float, u << 16); }
__device__ __forceinline__ float bfhi(u32 u) { return __builtin_bit_cast(float, u & 0xffff0000u); }
__device__ __forceinline__ float rbf(float f) { return (float)(__bf16)f; }
__device__ __forceinline__ u32 f2bf(float f) { return (u32)__builtin_bit_cast(u16, (__bf16)f); }
__device__ __forceinline__ u32 pkbf(float lo, float hi) {
  typedef __bf16 bf2 __attribute__((ext_vector_type(2)));
  typedef float f2 __attribute__((ext_vector_type(2)));
  f2 v = {lo, hi};
  return __builtin_bit_cast(u32, __builtin_convertvector(v, bf2));
}
__device__ __forceinline__ f32x4 mfma32(u32x4 a, u32x4 b, f32x4 c) {
  return __builtin_amdgcn_mfma_f32_16x16x32_bf16(__builtin_bit_cast(bf16x8, a), __builtin_bit_cast(bf16x8, b), c, 0, 0, 0);
}
__device__ __forceinline__ f32x4 mfma16(u32x2 a, u32x2 b, f32x4 c) {
  return __builtin_amdgcn_mfma_f32_16x16x16bf16_1k(__builtin_bit_cast(bf16x4, a), __builtin_bit_cast(bf16x4, b), c, 0, 0, 0);
}
template <int CTRL> __device__ __forceinline__ float dpp(float x) {
  return __builtin_bit_cast(float, __builtin_amdgcn_mov_dpp(__builtin_bit_cast(int, x), CTRL, 0xf, 0xf, true));
}
constexpr int XOR1 = 0xB1, XOR2 = 0x4E, XOR7 = 0x141, XOR8 = 0x128, ROR4 = 0x124;
__device__ __forceinline__ float fold32(float a, float b) {
  auto r = __builtin_amdgcn_permlane32_swap(__builtin_bit_cast(u32, a), __builtin_bit_cast(u32, b), false, false);
  return __builtin_bit_cast(float, (u32)r[0]) + __builtin_bit_cast(float, (u32)r[1]);
}
__device__ __forceinline__ float fold16(float a, float b) {
  auto r = __builtin_amdgcn_permlane16_swap(__builtin_bit_cast(u32, a), __builtin_bit_cast(u32, b), false, false);
  return __builtin_bit_cast(float, (u32)r[0]) + __builtin_bit_cast(float, (u32)r[1]);
}
__device__ __forceinline__ float max32(float a) {
  auto r = __builtin_amdgcn_permlane32_swap(__builtin_bit_cast(u32, a), __builtin_bit_cast(u32, a), false, false);
  return fmaxf(__builtin_bit_cast(float, (u32)r[0]), __builtin_bit_cast(float, (u32)r[1]));
}
__device__ __forceinline__ float max16(float a) {
  auto r = __builtin_amdgcn_permlane16_swap(__builtin_bit_cast(u32, a), __builtin_bit_cast(u32, a), false, false);
  return fmaxf(__builtin_bit_cast(float, (u32)r[0]), __builtin_bit_cast(float, (u32)r[1]));
}
__device__ __forceinline__ float wave_sum(float v) {
  v += dpp<XOR1>(v); v += dpp<XOR2>(v); v += dpp<XOR7>(v); v += dpp<XOR8>(v);
  v = fold16(v, v); v = fold32(v, v);
  return v;
}
__device__ __forceinline__ float wave_max(float v) {
  v = fmaxf(v, dpp<XOR1>(v)); v = fmaxf(v, dpp<XOR2>(v)); v = fmaxf(v, dpp<XOR7>(v)); v = fmaxf(v, dpp<XOR8>(v));
  return max32(max16(v));
}
__device__ __forceinline__ float reduce8(const float (&v)[8], int l) {
  float a[4], b[2];
#pragma unroll
  for (int j = 0; j < 4; ++j) a[j] = fold32(v[j], v[j + 4]);
#pragma unroll
  for (int j = 0; j < 2; ++j) b[j] = fold16(a[j], a[j + 2]);
  const bool hi = (l & 8) != 0;
  const float keep = hi ? b[1] : b[0], send = hi ? b[0] : b[1];
  return keep + dpp<XOR8>(send);
}
__device__ __forceinline__ u32x4 ldnt(const unsigned char* p) { return __builtin_nontemporal_load((const u32x4*)p); }
#define SBAR() __builtin_amdgcn_sched_barrier(0)
__device__ __forceinline__ void wg_barrier() {
  asm volatile("s_waitcnt lgkmcnt(0)" ::: "memory");
  __builtin_amdgcn_s_barrier();
  asm volatile("" ::: "memory");
}
__device__ __forceinline__ u32 enc24(float v) { return (__builtin_bit_cast(u32, v) + 0x80u) >> 8; }

#define STAMP(i) do { } while (0)

struct Unit { u32x4 k[4]; u32x4 v[4]; };

__global__ void __launch_bounds__(NT, 1) fused_decode(Params p) {
  __shared__ __attribute__((aligned(16))) unsigned char smem[L_BYTES];
  const int tid = threadIdx.x, l = tid & 63, w = __builtin_amdgcn_readfirstlane(tid >> 6);
  const int b = blockIdx.x, g = b >> 6, s = b & 63;
  const __amdgpu_buffer_rsrc_t rs = __builtin_amdgcn_make_buffer_rsrc((void*)p.ws, 0, (int)WS_END, 0x00020000);
  STAMP(0);
  u64 E;
  const int g4 = l >> 4, c2 = (l >> 3) & 1;
  const __amdgpu_buffer_rsrc_t rk = __builtin_amdgcn_make_buffer_rsrc((void*)(p.K + (size_t)g * SK * DH), 0, SK * DH * 2, 0x00020000);
  const __amdgpu_buffer_rsrc_t rv = __builtin_amdgcn_make_buffer_rsrc((void*)(p.V + (size_t)g * SK * DH), 0, SK * DH * 2, 0x00020000);
  const u32 koff = (u32)(((l & 15) >> 1) * 256 + (2 * g4 + (l & 1)) * 16);
  const u32 voff = (u32)(g4 * 512 + (l & 15) * 16);
  auto issueK = [&](Unit& U, u32 t) {
    SBAR();
    const int o = (int)(koff + (t < (u32)NUNIT_POOL ? (4 * t + w) * 4096u : 0x40000000u));
    U.k[0] = __builtin_amdgcn_raw_buffer_load_b128(rk, o, 0, 2); U.k[1] = __builtin_amdgcn_raw_buffer_load_b128(rk, o + 128, 0, 2);
    U.k[2] = __builtin_amdgcn_raw_buffer_load_b128(rk, o + 2048, 0, 2); U.k[3] = __builtin_amdgcn_raw_buffer_load_b128(rk, o + 2048 + 128, 0, 2);
    SBAR();
  };
  auto issueV = [&](Unit& U, u32 t) {
    SBAR();
    const int o = (int)(voff + (t < (u32)NUNIT_POOL ? (4 * t + w) * 4096u : 0x40000000u));
    U.v[0] = __builtin_amdgcn_raw_buffer_load_b128(rv, o, 0, 2); U.v[1] = __builtin_amdgcn_raw_buffer_load_b128(rv, o + 256, 0, 2);
    U.v[2] = __builtin_amdgcn_raw_buffer_load_b128(rv, o + 2048, 0, 2); U.v[3] = __builtin_amdgcn_raw_buffer_load_b128(rv, o + 2048 + 256, 0, 2);
    SBAR();
  };
  Unit A, B;
  u32 tagl, tagh;

  if (s < 44) {
    const bool isq = s < 16;
    const int t = g * 28 + (s - 16), mat = t / 56, tt = t % 56, cgp = tt >> 2, ks = tt & 3;
    const unsigned char* wb = isq ? (const unsigned char*)(p.wq + g * 1024 + s * 64) : (const unsigned char*)((mat ? p.wu : p.wg) + cgp * 256 + w * 64);
    const u32 ldB = isq ? 8192u : 7168u;
    const int R0 = 256 * (isq ? w : ks);
    const u32x4 xa = *(const u32x4*)(p.x + 16 * l), xb = *(const u32x4*)(p.x + 16 * l + 8);
    const u32x2 xr = *(const u32x2*)(p.x + R0 + 4 * l);
    const u32 toff = (u32)(4 * (l >> 3)) * ldB + (u32)(l & 7) * 16u;
    const unsigned char* trow = wb + (size_t)R0 * ldB;
    SBAR();
    float acc[8];
#pragma unroll
    for (int j = 0; j < 8; ++j) acc[j] = 0.f;
    auto tload = [&](int i) -> u32x4 { return ldnt(trow + (size_t)(32 * (i >> 2) + (i & 3)) * ldB + toff); };
    auto lnorm = [&]() {
      float xs[16];
#pragma unroll
      for (int j = 0; j < 4; ++j) { xs[2 * j] = bflo(xa[j]); xs[2 * j + 1] = bfhi(xa[j]); xs[8 + 2 * j] = bflo(xb[j]); xs[9 + 2 * j] = bfhi(xb[j]); }
      float sm = 0.f;
#pragma unroll
      for (int j = 0; j < 16; ++j) sm += xs[j];
      const float mean = wave_sum(sm) * (1.f / DM);
      float sv = 0.f;
#pragma unroll
      for (int j = 0; j < 16; ++j) { const float d = xs[j] - mean; sv += d * d; }
      const float var = wave_sum(sv) * (1.f / DM);
      const float mb = rbf(mean), den = rbf(sqrtf(rbf(rbf(var) + 1e-5f)));
      f32x4 xn;
      xn[0] = rbf(rbf(bflo(xr[0]) - mb) / den); xn[1] = rbf(rbf(bfhi(xr[0]) - mb) / den);
      xn[2] = rbf(rbf(bflo(xr[1]) - mb) / den); xn[3] = rbf(rbf(bfhi(xr[1]) - mb) / den);
      *(f32x4*)(smem + L_XN + w * 1024 + l * 16) = xn;
    };
    auto fma8 = [&](const u32x4& wv, float xv) {
#pragma unroll
      for (int j = 0; j < 4; ++j) { acc[2 * j] = fmaf(xv, bflo(wv[j]), acc[2 * j]); acc[2 * j + 1] = fmaf(xv, bfhi(wv[j]), acc[2 * j + 1]); }
    };
    u32x4 T[32];
#pragma unroll
    for (int i = 0; i < 8; ++i) T[i] = tload(i);
    SBAR();
    E = __hip_atomic_load((gu64*)(p.ws + WS_EPOCH), RLX_AGENT);
    SBAR();
    if (isq) {
#pragma unroll
      for (int i = 8; i < 32; ++i) T[i] = tload(i);
      SBAR();
      issueK(A, s); issueV(A, s); issueK(B, s + 64); issueV(B, s + 64);
    }
    asm volatile("" ::: "memory");
    SBAR();
    lnorm();
    if (isq) {
#pragma unroll
      for (int ib = 0; ib < 8; ++ib) {
        const f32x4 xv = *(const f32x4*)(smem + L_XN + w * 1024 + (32 * ib + 4 * (l >> 3)) * 4);
#pragma unroll
        for (int u = 0; u < 4; ++u) fma8(T[4 * ib + u], xv[u]);
      }
    } else {
#pragma unroll
      for (int ib = 0; ib < 8; ++ib) {
        const f32x4 xv = *(const f32x4*)(smem + L_XN + w * 1024 + (32 * ib + 4 * (l >> 3)) * 4);
#pragma unroll
        for (int u = 0; u < 4; ++u) {
          const int i = 4 * ib + u;
          fma8(T[i & 7], xv[u]);
          SBAR();
          if (i + 8 < 32) T[i & 7] = tload(i + 8);
          SBAR();
        }
        if (ib == 5) issueK(A, s);
        if (ib == 6) issueV(A, s);
        if (ib == 7) { issueK(B, s + 64); issueV(B, s + 64); }
      }
    }
    const float r = reduce8(acc, l);
    const int col = 8 * (l & 7) + 4 * (l >> 5) + 2 * ((l >> 4) & 1) + ((l >> 3) & 1);
    {
      const u64 tg = E + 1;
      tagl = (u32)tg << 24; tagh = (u32)(tg >> 8);
    }
    if (isq) {
      *(float*)(smem + L_QP + (w * 64 + col) * 4) = r;
      wg_barrier();
      if (w == 0) {
        const float* qp = (const float*)(smem + L_QP);
        const float qv = rbf((qp[l] + qp[64 + l]) + (qp[128 + l] + qp[192 + l]));
        const u32x2 gr = {(__builtin_bit_cast(u32, qv) >> 8) | tagl, tagh};
        __builtin_amdgcn_raw_buffer_store_b64(gr, rs, (int)(WS_Q + (g * 1024 + s * 64 + l) * 8), 0, 16);
      }
    } else {
      const u32x2 gr = {enc24(r) | tagl, tagh};
      __builtin_amdgcn_raw_buffer_store_b64(gr, rs, (int)(WS_GU + ((mat * 4 + ks) * FF + cgp * 256 + w * 64 + col) * 8), 0, 16);
    }
  } else {
    issueK(A, s); issueV(A, s); issueK(B, s + 64); issueV(B, s + 64);
    E = __hip_atomic_load((gu64*)(p.ws + WS_EPOCH), RLX_AGENT);
    const u64 tg = E + 1;
    tagl = (u32)tg << 24; tagh = (u32)(tg >> 8);
  }
  STAMP(1);
  auto bad16 = [&](const u32x4& x) -> u32 { return (x[1] ^ tagh) | (x[3] ^ tagh) | (((x[0] ^ tagl) | (x[2] ^ tagl)) >> 24); };

  {
    const int off = (int)(WS_Q + (g * 1024 + w * 256 + 2 * l) * 8);
    u32x4 q0, q1;
    for (u32 spin = 0; spin < SPIN_CAP; ++spin) {
      q0 = __builtin_amdgcn_raw_buffer_load_b128(rs, off, 0, 16);
      q1 = __builtin_amdgcn_raw_buffer_load_b128(rs, off + 1024, 0, 16);
      if (!__any(bad16(q0) | bad16(q1))) break;
      __builtin_amdgcn_s_sleep(2);
    }
    u32* qs = (u32*)(smem + L_QS);
    qs[w * 128 + l] = ((q0[0] >> 8) & 0xffffu) | ((q0[2] << 8) & 0xffff0000u);
    qs[w * 128 + 64 + l] = ((q1[0] >> 8) & 0xffffu) | ((q1[2] << 8) & 0xffff0000u);
  }
  wg_barrier();
  STAMP(2);
  u32x4 qf[2];
#pragma unroll
  for (int s2 = 0; s2 < 2; ++s2) qf[s2] = *(const u32x4*)(smem + L_QS + ((l & 7) * 128 + (8 * s2 + 2 * g4 + c2) * 8) * 2);

  float m = -3.0e38f, lsum = 0.f;
  f32x4 acc[8];
#pragma unroll
  for (int j = 0; j < 8; ++j) acc[j] = f32x4{0.f, 0.f, 0.f, 0.f};
  u32x2 pa;
  auto qk = [&](const Unit& U, bool valid) {
    const f32x4 z = {0.f, 0.f, 0.f, 0.f};
    f32x4 d0 = mfma32(U.k[0], qf[0], z); d0 = mfma32(U.k[1], qf[1], d0);
    f32x4 d1 = mfma32(U.k[2], qf[0], z); d1 = mfma32(U.k[3], qf[1], d1);
    float t0 = (c2 ? d0[3] : d0[0]) + dpp<XOR8>(c2 ? d0[1] : d0[2]);
    float t1 = (c2 ? d1[3] : d1[0]) + dpp<XOR8>(c2 ? d1[1] : d1[2]);
    t0 = rbf(rbf(t0) * 0.08838834764831845f) * 1.4426950408889634f;
    t1 = rbf(rbf(t1) * 0.08838834764831845f) * 1.4426950408889634f;
    if (!valid) { t0 = -3.0e38f; t1 = -3.0e38f; }
    const float mx = fmaxf(t0, t1);
    if (__any(mx > m)) {
      float mn = fmaxf(mx, dpp<XOR8>(mx));
      mn = fmaxf(max32(max16(mn)), m);
      const float al = __builtin_amdgcn_exp2f(m - mn);
      m = mn; lsum *= al;
      float ar[4];
#pragma unroll
      for (int r = 0; r < 4; ++r) {
        const float lo = __builtin_bit_cast(float, __builtin_amdgcn_readlane(__builtin_bit_cast(int, al), r));
        const float hi = __builtin_bit_cast(float, __builtin_amdgcn_readlane(__builtin_bit_cast(int, al), 4 + r));
        ar[r] = (g4 & 1) ? hi : lo;
      }
#pragma unroll
      for (int j = 0; j < 8; ++j)
#pragma unroll
        for (int r = 0; r < 4; ++r) acc[j][r] *= ar[r];
    }
    const float p0 = __builtin_amdgcn_exp2f(t0 - m), p1 = __builtin_amdgcn_exp2f(t1 - m);
    lsum += p0 + p1;
    const u32 pk = pkbf(p0, p1);
    pa[0] = c2 ? 0u : pk; pa[1] = c2 ? pk : 0u;
  };
  auto pv = [&](const Unit& U) {
#pragma unroll
    for (int j = 0; j < 8; ++j) {
      const u32 sel = (j & 1) ? 0x07060302u : 0x05040100u;
      u32x2 f;
      f[0] = __builtin_amdgcn_perm(U.v[2][j >> 1], U.v[0][j >> 1], sel);
      f[1] = __builtin_amdgcn_perm(U.v[3][j >> 1], U.v[1][j >> 1], sel);
      acc[j] = mfma16(pa, f, acc[j]);
    }
  };
  const int rgl = s >> 2, cg = s & 3, RG = g * 16 + rgl;
  u32x4 Fd[7], Fo[8];
  auto issueF = [&]() {
    const unsigned char* db = (const unsigned char*)(p.wd + (size_t)(56 * RG) * DM + 256 * cg + 64 * w) + (u32)(7 * (l >> 3)) * 2048u + (u32)(l & 7) * 16u;
#pragma unroll
    for (int i = 0; i < 7; ++i) Fd[i] = ldnt(db + i * 2048);
    const unsigned char* ob = (const unsigned char*)(p.wo + (size_t)(64 * RG) * DM + 256 * cg + 64 * w) + (u32)(4 * (l >> 3)) * 2048u + (u32)(l & 7) * 16u;
#pragma unroll
    for (int i = 0; i < 8; ++i) Fo[i] = ldnt(ob + (32 * (i >> 2) + (i & 3)) * 2048);
    SBAR();
  };
  u32x2 gx[8];
  const int fidx = 56 * RG + (l < 56 ? l : 0);
  auto issue_gu = [&]() {
#pragma unroll
    for (int i = 0; i < 8; ++i) gx[i] = __builtin_amdgcn_raw_buffer_load_b64(rs, (int)(WS_GU + (i * FF + fidx) * 8), 0, 16);
    SBAR();
  };
  auto compute_h = [&]() {
    auto gu_bad = [&]() -> bool {
      u32 bad = 0;
#pragma unroll
      for (int i = 0; i < 8; ++i) bad |= (gx[i][1] ^ tagh) | ((gx[i][0] ^ tagl) >> 24);
      return __any(bad);
    };
    if (gu_bad()) {
      for (u32 spin = 0; spin < SPIN_CAP; ++spin) {
        __builtin_amdgcn_s_sleep(2);
        issue_gu();
        if (!gu_bad()) break;
      }
    }
    float a[8];
#pragma unroll
    for (int i = 0; i < 8; ++i) a[i] = __builtin_bit_cast(float, gx[i][0] << 8);
    const float gt = rbf((a[0] + a[1]) + (a[2] + a[3])), up = rbf((a[4] + a[5]) + (a[6] + a[7]));
    const float ge = rbf(0.5f * gt * (1.f + tanhf(0.7978845608028654f * (gt + 0.044715f * gt * gt * gt))));
    if (l < 56) *(float*)(smem + L_H + ((l / 7) * 8 + (l % 7)) * 4) = rbf(ge * up);
  };
  gu32* cnt = (gu32*)(p.ws + WS_CNT + (g * 4 + w) * 256);
  auto ticket = [&](int k) -> u32 {
    return (u32)(64 * k + s);
  };
#define TICKET_GET(v, N) (v)
  {
    u32 nA = s + 128, nB = s + 192;
    int kk = 0;
    u32 vA = ticket(kk++), vB = ticket(kk++);
    SBAR();
    bool bvalid = true;
    bool first = true;
    do {
      qk(A, true);
      issueK(A, nA);
      pv(A);
      issueV(A, nA);
      first = false;
      nA = PSTAT * 64 + TICKET_GET(vA, 16);
      vA = ticket(kk++);
      SBAR();
      qk(B, true);
      issueK(B, nB);
      pv(B);
      issueV(B, nB);
      bvalid = nB < (u32)NUNIT_POOL;
      nB = PSTAT * 64 + TICKET_GET(vB, 16);
      vB = ticket(kk++);
      SBAR();
    } while (nA < (u32)NUNIT_POOL);
    if (w == 3) issue_gu();
    issueF();
    qk(A, true); pv(A);
    qk(B, bvalid); pv(B);
  }
  STAMP(3);

  {
    float ls = lsum + dpp<XOR8>(lsum);
    ls = fold16(ls, ls); ls = fold32(ls, ls);
    if (l < 8) { *(float*)(smem + L_SM + (w * 8 + l) * 4) = m; *(float*)(smem + L_SL + (w * 8 + l) * 4) = ls; }
#pragma unroll
    for (int r = 0; r < 4; ++r) {
      f32x4 o;
#pragma unroll
      for (int j = 0; j < 4; ++j) o[j] = fold32(acc[j][r], acc[j + 4][r]);
      *(f32x4*)(smem + L_SO + ((w * 8 + 4 * (g4 & 1) + r) * 128 + 8 * (l & 15) + 4 * (l >> 5)) * 4) = o;
    }
  }
  wg_barrier();
  STAMP(4);
  {
    const int hd = 2 * w + (l >> 5), e = l & 31;
    const float* sm = (const float*)(smem + L_SM);
    const float* sl = (const float*)(smem + L_SL);
    const float m0 = sm[hd], m1 = sm[8 + hd], m2 = sm[16 + hd], m3 = sm[24 + hd];
    const float M = fmaxf(fmaxf(m0, m1), fmaxf(m2, m3));
    const float w0 = __builtin_amdgcn_exp2f(m0 - M), w1 = __builtin_amdgcn_exp2f(m1 - M), w2 = __builtin_amdgcn_exp2f(m2 - M), w3 = __builtin_amdgcn_exp2f(m3 - M);
    const float L = (w0 * sl[hd] + w1 * sl[8 + hd]) + (w2 * sl[16 + hd] + w3 * sl[24 + hd]);
    const float inv = 1.f / L;
    const f32x4* so = (const f32x4*)(smem + L_SO);
    const f32x4 o = (so[(0 * 8 + hd) * 32 + e] * w0 + so[(1 * 8 + hd) * 32 + e] * w1) + (so[(2 * 8 + hd) * 32 + e] * w2 + so[(3 * 8 + hd) * 32 + e] * w3);
    const int off = (int)(WS_O + ((((g * 8 + hd) * 2 + (e >> 4)) * 64 + s) * 64 + 4 * (e & 15)) * 8);
    const u32x4 g0 = {enc24(o[0] * inv) | tagl, tagh, enc24(o[1] * inv) | tagl, tagh};
    const u32x4 g1 = {enc24(o[2] * inv) | tagl, tagh, enc24(o[3] * inv) | tagl, tagh};
    __builtin_amdgcn_raw_buffer_store_b128(g0, rs, off, 0, 16);
    __builtin_amdgcn_raw_buffer_store_b128(g1, rs, off + 16, 0, 16);
    if (e == 0) {
      const float lse = M + __builtin_amdgcn_logf(L);
      const u32 hi = enc24(lse);
      const float lo = lse - __builtin_bit_cast(float, hi << 8);
      const u32x4 gl = {hi | tagl, tagh, enc24(lo) | tagl, tagh};
      __builtin_amdgcn_raw_buffer_store_b128(gl, rs, (int)(WS_LSE + ((g * 8 + hd) * 64 + s) * 16), 0, 16);
    }
  }
  SBAR();
  const u32x2 xfin = *(const u32x2*)(p.x + 4 * b);
  if (w == 3) compute_h();
  wg_barrier();
  float oacc[8];
#pragma unroll
  for (int j = 0; j < 8; ++j) oacc[j] = 0.f;
  {
    const f32x4 h0 = *(const f32x4*)(smem + L_H + (l >> 3) * 32), h1 = *(const f32x4*)(smem + L_H + (l >> 3) * 32 + 16);
#pragma unroll
    for (int i = 0; i < 7; ++i) {
      const float hv = i < 4 ? h0[i & 3] : h1[i & 3];
#pragma unroll
      for (int j = 0; j < 4; ++j) { oacc[2 * j] = fmaf(hv, bflo(Fd[i][j]), oacc[2 * j]); oacc[2 * j + 1] = fmaf(hv, bfhi(Fd[i][j]), oacc[2 * j + 1]); }
    }
  }
  STAMP(5);
  {
    const int hd = rgl >> 1, half = rgl & 1;
    const int offl = (int)(WS_LSE + ((g * 8 + hd) * 64 + l) * 16);
    const int offo = (int)(WS_O + ((((g * 8 + hd) * 2 + half) * 64 + 16 * w + (l >> 5)) * 64 + 2 * (l & 31)) * 8);
    u32x4 xl, xo[8];
    u32 pend = 0x1ffu;
    for (u32 spin = 0; spin < SPIN_CAP; ++spin) {
      if (pend & 0x100u) xl = __builtin_amdgcn_raw_buffer_load_b128(rs, offl, 0, 16);
#pragma unroll
      for (int i = 0; i < 8; ++i)
        if (pend & (1u << i)) xo[i] = __builtin_amdgcn_raw_buffer_load_b128(rs, offo + i * 1024, 0, 16);
      u32 np = __any(bad16(xl)) ? 0x100u : 0u;
#pragma unroll
      for (int i = 0; i < 8; ++i) np |= __any(bad16(xo[i])) ? (1u << i) : 0u;
      pend = np;
      if (!pend) break;
      __builtin_amdgcn_s_sleep(1);
    }
    const float lse = __builtin_bit_cast(float, xl[0] << 8) + __builtin_bit_cast(float, xl[2] << 8);
    const float M = wave_max(lse);
    const float wt = __builtin_amdgcn_exp2f(lse - M);
    const float wn = wt / wave_sum(wt);
    float o0 = 0.f, o1 = 0.f;
#pragma unroll
    for (int i = 0; i < 8; ++i) {
      const float wa = __builtin_bit_cast(float, __builtin_amdgcn_readlane(__builtin_bit_cast(int, wn), 16 * w + 2 * i));
      const float wb = __builtin_bit_cast(float, __builtin_amdgcn_readlane(__builtin_bit_cast(int, wn), 16 * w + 2 * i + 1));
      const float ws_ = (l >> 5) ? wb : wa;
      o0 = fmaf(ws_, __builtin_bit_cast(float, xo[i][0] << 8), o0);
      o1 = fmaf(ws_, __builtin_bit_cast(float, xo[i][2] << 8), o1);
    }
    *(float*)(smem + L_OP + (w * 64 + 2 * (l & 31) + (l >> 5)) * 4) = fold32(o0, o1);
  }
  wg_barrier();
  STAMP(6);
  {
    const f32x4* op = (const f32x4*)(smem + L_OP);
#pragma unroll
    for (int ib = 0; ib < 2; ++ib) {
      const int idx = 8 * ib + (l >> 3);
      const f32x4 ov = (op[idx] + op[16 + idx]) + (op[32 + idx] + op[48 + idx]);
#pragma unroll
      for (int u = 0; u < 4; ++u) {
        const float o = rbf(ov[u]);
        const u32x4 wv = Fo[4 * ib + u];
#pragma unroll
        for (int j = 0; j < 4; ++j) { oacc[2 * j] = fmaf(o, bflo(wv[j]), oacc[2 * j]); oacc[2 * j + 1] = fmaf(o, bfhi(wv[j]), oacc[2 * j + 1]); }
      }
    }
    const float r = reduce8(oacc, l);
    const int col = 256 * cg + 64 * w + 8 * (l & 7) + 4 * (l >> 5) + 2 * ((l >> 4) & 1) + ((l >> 3) & 1);
    const u32x2 gr = {enc24(r) | tagl, tagh};
    __builtin_amdgcn_raw_buffer_store_b64(gr, rs, (int)(WS_IN + (((col >> 2) * 64 + RG) * 4 + (col & 3)) * 8), 0, 16);
  }
  STAMP(7);
  if (w == 0) {
    const int off = (int)(WS_IN + (b * 64 + l) * 32);
    u32x4 y0, y1;
    for (u32 spin = 0; spin < SPIN_CAP; ++spin) {
      y0 = __builtin_amdgcn_raw_buffer_load_b128(rs, off, 0, 16);
      y1 = __builtin_amdgcn_raw_buffer_load_b128(rs, off + 16, 0, 16);
      if (!__any(bad16(y0) | bad16(y1))) break;
      __builtin_amdgcn_s_sleep(1);
    }
    const float v0 = __builtin_bit_cast(float, y0[0] << 8), v1 = __builtin_bit_cast(float, y0[2] << 8);
    const float v2 = __builtin_bit_cast(float, y1[0] << 8), v3 = __builtin_bit_cast(float, y1[2] << 8);
    const float a0 = fold32(v0, v2), a1 = fold32(v1, v3);
    float r = fold16(a0, a1);
    r += dpp<XOR1>(r); r += dpp<XOR2>(r); r += dpp<XOR7>(r); r += dpp<XOR8>(r);
    if ((l & 15) == 0) {
      const u32 xw = (g4 & 2) ? xfin[1] : xfin[0];
      const float xv = (g4 & 1) ? bfhi(xw) : bflo(xw);
      p.out[4 * b + g4] = (u16)f2bf(xv + r);
    }
    if (b == 0 && l == 0) __hip_atomic_store((gu64*)(p.ws + WS_EPOCH), E + 1, RLX_AGENT);
  }
  STAMP(8);
}

extern "C" void kernel_launch(void* const* d_in, const int* in_sizes, int n_in, void* d_out, int out_size, void* d_ws, size_t ws_size,
                              hipStream_t stream) {
  Params p;
  p.x = (const u16*)d_in[0];
  p.K = (const u16*)d_in[1];
  p.V = (const u16*)d_in[2];
  p.wq = (const u16*)d_in[3];
  p.wo = (const u16*)d_in[4];
  p.wg = (const u16*)d_in[6];
  p.wu = (const u16*)d_in[7];
  p.wd = (const u16*)d_in[8];
  p.out = (u16*)d_out;
  p.ws = (unsigned char*)d_ws;
  fused_decode<<<NWG, NT, 0, stream>>>(p);
}
```

```cpp
#include <hip/hip_runtime.h>
#include <hip/hip_bf16.h>
#include <stdint.h>

#define DYN 0
#define AUXKV 18
#define WNT 1

typedef unsigned u32;
typedef unsigned long long u64;
typedef unsigned short u16;
typedef u32 u32x4 __attribute__((ext_vector_type(4)));
typedef u32 u32x2 __attribute__((ext_vector_type(2)));
typedef float f32x4 __attribute__((ext_vector_type(4)));
typedef short bf16x8 __attribute__((ext_vector_type(8)));
typedef short bf16x4 __attribute__((ext_vector_type(4)));
#define GAS __attribute__((address_space(1)))
typedef GAS u64 gu64;
typedef GAS u32 gu32;
#define RLX_AGENT __ATOMIC_RELAXED, __HIP_MEMORY_SCOPE_AGENT

constexpr int DM = 1024, SK = 65536, DH = 128, FF = 3584;
constexpr int NWG = 256, NT = 256;
constexpr int PSTAT = 4;
constexpr int NUNIT_POOL = 1024;
constexpr u32 WS_EPOCH = 0;
constexpr u32 WS_CNT = 4096;
constexpr u32 WS_Q = 16384;
constexpr u32 WS_GU = WS_Q + 4 * 1024 * 8;
constexpr u32 WS_LSE = WS_GU + 2 * 4 * FF * 8;
constexpr u32 WS_O = WS_LSE + 4 * 8 * 64 * 16;
constexpr u32 WS_IN = WS_O + 4 * 8 * 2 * 64 * 64 * 8;
constexpr u32 WS_END = WS_IN + 256 * 64 * 4 * 8;
constexpr u32 WS_DBG = WS_END;
constexpr u32 SPIN_CAP = 1u << 17;
constexpr int L_XN = 0;
constexpr int L_QP = L_XN + 4096;
constexpr int L_QS = L_QP + 1024;
constexpr int L_SM = L_QS + 2048;
constexpr int L_SL = L_SM + 128;
constexpr int L_H = L_SL + 128;
constexpr int L_OP = L_H + 256;
constexpr int L_SO = L_OP + 1024;
constexpr int L_BYTES = 88 * 1024;

struct Params {
  const u16* x; const u16* K; const u16* V; const u16* wq; const u16* wo; const u16* wg; const u16* wu; const u16* wd;
  u16* out; unsigned char* ws;
};

__device__ __forceinline__ float bflo(u32 u) { return __builtin_bit_cast(float, u << 16); }
__device__ __forceinline__ float bfhi(u32 u) { return __builtin_bit_cast(float, u & 0xffff0000u); }
__device__ __forceinline__ float rbf(float f) { return (float)(__bf16)f; }
__device__ __forceinline__ u32 f2bf(float f) { return (u32)__builtin_bit_cast(u16, (__bf16)f); }
__device__ __forceinline__ u32 pkbf(float lo, float hi) {
  typedef __bf16 bf2 __attribute__((ext_vector_type(2)));
  typedef float f2 __attribute__((ext_vector_type(2)));
  f2 v = {lo, hi};
  return __builtin_bit_cast(u32, __builtin_convertvector(v, bf2));
}
__device__ __forceinline__ f32x4 mfma32(u32x4 a, u32x4 b, f32x4 c) {
  return __builtin_amdgcn_mfma_f32_16x16x32_bf16(__builtin_bit_cast(bf16x8, a), __builtin_bit_cast(bf16x8, b), c, 0, 0, 0);
}
__device__ __forceinline__ f32x4 mfma16(u32x2 a, u32x2 b, f32x4 c) {
  return __builtin_amdgcn_mfma_f32_16x16x16bf16_1k(__builtin_bit_cast(bf16x4, a), __builtin_bit_cast(bf16x4, b), c, 0, 0, 0);
}
template <int CTRL> __device__ __forceinline__ float dpp(float x) {
  return __builtin_bit_cast(float, __builtin_amdgcn_mov_dpp(__builtin_bit_cast(int, x), CTRL, 0xf, 0xf, true));
}
constexpr int XOR1 = 0xB1, XOR2 = 0x4E, XOR7 = 0x141, XOR8 = 0x128, ROR4 = 0x124;
__device__ __forceinline__ float fold32(float a, float b) {
  auto r = __builtin_amdgcn_permlane32_swap(__builtin_bit_cast(u32, a), __builtin_bit_cast(u32, b), false, false);
  return __builtin_bit_cast(float, (u32)r[0]) + __builtin_bit_cast(float, (u32)r[1]);
}
__device__ __forceinline__ float fold16(float a, float b) {
  auto r = __builtin_amdgcn_permlane16_swap(__builtin_bit_cast(u32, a), __builtin_bit_cast(u32, b), false, false);
  return __builtin_bit_cast(float, (u32)r[0]) + __builtin_bit_cast(float, (u32)r[1]);
}
__device__ __forceinline__ float max32(float a) {
  auto r = __builtin_amdgcn_permlane32_swap(__builtin_bit_cast(u32, a), __builtin_bit_cast(u32, a), false, false);
  return fmaxf(__builtin_bit_cast(float, (u32)r[0]), __builtin_bit_cast(float, (u32)r[1]));
}
__device__ __forceinline__ float max16(float a) {
  auto r = __builtin_amdgcn_permlane16_swap(__builtin_bit_cast(u32, a), __builtin_bit_cast(u32, a), false, false);
  return fmaxf(__builtin_bit_cast(float, (u32)r[0]), __builtin_bit_cast(float, (u32)r[1]));
}
__device__ __forceinline__ float wave_sum(float v) {
  v += dpp<XOR1>(v); v += dpp<XOR2>(v); v += dpp<XOR7>(v); v += dpp<XOR8>(v);
  v = fold16(v, v); v = fold32(v, v);
  return v;
}
__device__ __forceinline__ float wave_max(float v) {
  v = fmaxf(v, dpp<XOR1>(v)); v = fmaxf(v, dpp<XOR2>(v)); v = fmaxf(v, dpp<XOR7>(v)); v = fmaxf(v, dpp<XOR8>(v));
  return max32(max16(v));
}
__device__ __forceinline__ float reduce8(const float (&v)[8], int l) {
  float a[4], b[2];
#pragma unroll
  for (int j = 0; j < 4; ++j) a[j] = fold32(v[j], v[j + 4]);
#pragma unroll
  for (int j = 0; j < 2; ++j) b[j] = fold16(a[j], a[j + 2]);
  const bool hi = (l & 8) != 0;
  const float keep = hi ? b[1] : b[0], send = hi ? b[0] : b[1];
  return keep + dpp<XOR8>(send);
}
__device__ __forceinline__ u32x4 ldnt(const unsigned char* p) { return WNT ? __builtin_nontemporal_load((const u32x4*)p) : *(const u32x4*)p; }
#define SBAR() __builtin_amdgcn_sched_barrier(0)
__device__ __forceinline__ void wg_barrier() {
  asm volatile("s_waitcnt lgkmcnt(0)" ::: "memory");
  __builtin_amdgcn_s_barrier();
  asm volatile("" ::: "memory");
}
__device__ __forceinline__ u32 enc24(float v) { return (__builtin_bit_cast(u32, v) + 0x80u) >> 8; }

#define STAMP(i) do { } while (0)

struct Unit { u32x4 k[4]; u32x4 v[4]; };

__global__ void __launch_bounds__(NT, 1) fused_decode(Params p) {
  __shared__ __attribute__((aligned(16))) unsigned char smem[L_BYTES];
  const int tid = threadIdx.x, l = tid & 63, w = __builtin_amdgcn_readfirstlane(tid >> 6);
  const int b = blockIdx.x, g = b >> 6, s = b & 63;
  const __amdgpu_buffer_rsrc_t rs = __builtin_amdgcn_make_buffer_rsrc((void*)p.ws, 0, (int)WS_END, 0x00020000);
  STAMP(0);
  u64 E;
  const int g4 = l >> 4, c2 = (l >> 3) & 1;
  const __amdgpu_buffer_rsrc_t rk = __builtin_amdgcn_make_buffer_rsrc((void*)(p.K + (size_t)g * SK * DH), 0, SK * DH * 2, 0x00020000);
  const __amdgpu_buffer_rsrc_t rv = __builtin_amdgcn_make_buffer_rsrc((void*)(p.V + (size_t)g * SK * DH), 0, SK * DH * 2, 0x00020000);
  const u32 koff = (u32)(((l & 15) >> 1) * 256 + (2 * g4 + (l & 1)) * 16);
  const u32 voff = (u32)(g4 * 512 + (l & 15) * 16);
  auto issueK = [&](Unit& U, u32 t) {
    SBAR();
    const int o = (int)(koff + (t < (u32)NUNIT_POOL ? (4 * t + w) * 4096u : 0x40000000u));
    U.k[0] = __builtin_amdgcn_raw_buffer_load_b128(rk, o, 0, AUXKV); U.k[1] = __builtin_amdgcn_raw_buffer_load_b128(rk, o + 128, 0, AUXKV);
    U.k[2] = __builtin_amdgcn_raw_buffer_load_b128(rk, o + 2048, 0, AUXKV); U.k[3] = __builtin_amdgcn_raw_buffer_load_b128(rk, o + 2048 + 128, 0, AUXKV);
    SBAR();
  };
  auto issueV = [&](Unit& U, u32 t) {
    SBAR();
    const int o = (int)(voff + (t < (u32)NUNIT_POOL ? (4 * t + w) * 4096u : 0x40000000u));
    U.v[0] = __builtin_amdgcn_raw_buffer_load_b128(rv, o, 0, AUXKV); U.v[1] = __builtin_amdgcn_raw_buffer_load_b128(rv, o + 256, 0, AUXKV);
    U.v[2] = __builtin_amdgcn_raw_buffer_load_b128(rv, o + 2048, 0, AUXKV); U.v[3] = __builtin_amdgcn_raw_buffer_load_b128(rv, o + 2048 + 256, 0, AUXKV);
    SBAR();
  };
  Unit A, B;
  u32 tagl, tagh;

  if (s < 44) {
    const bool isq = s < 16;
    const int t = g * 28 + (s - 16), mat = t / 56, tt = t % 56, cgp = tt >> 2, ks = tt & 3;
    const unsigned char* wb = isq ? (const unsigned char*)(p.wq + g * 1024 + s * 64) : (const unsigned char*)((mat ? p.wu : p.wg) + cgp * 256 + w * 64);
    const u32 ldB = isq ? 8192u : 7168u;
    const int R0 = 256 * (isq ? w : ks);
    const u32x4 xa = *(const u32x4*)(p.x + 16 * l), xb = *(const u32x4*)(p.x + 16 * l + 8);
    const u32x2 xr = *(const u32x2*)(p.x + R0 + 4 * l);
    const u32 toff = (u32)(4 * (l >> 3)) * ldB + (u32)(l & 7) * 16u;
    const unsigned char* trow = wb + (size_t)R0 * ldB;
    SBAR();
    float acc[8];
#pragma unroll
    for (int j = 0; j < 8; ++j) acc[j] = 0.f;
    auto tload = [&](int i) -> u32x4 { return ldnt(trow + (size_t)(32 * (i >> 2) + (i & 3)) * ldB + toff); };
    auto lnorm = [&]() {
      float xs[16];
#pragma unroll
      for (int j = 0; j < 4; ++j) { xs[2 * j] = bflo(xa[j]); xs[2 * j + 1] = bfhi(xa[j]); xs[8 + 2 * j] = bflo(xb[j]); xs[9 + 2 * j] = bfhi(xb[j]); }
      float sm = 0.f;
#pragma unroll
      for (int j = 0; j < 16; ++j) sm += xs[j];
      const float mean = wave_sum(sm) * (1.f / DM);
      float sv = 0.f;
#pragma unroll
      for (int j = 0; j < 16; ++j) { const float d = xs[j] - mean; sv += d * d; }
      const float var = wave_sum(sv) * (1.f / DM);
      const float mb = rbf(mean), den = rbf(sqrtf(rbf(rbf(var) + 1e-5f)));
      f32x4 xn;
      xn[0] = rbf(rbf(bflo(xr[0]) - mb) / den); xn[1] = rbf(rbf(bfhi(xr[0]) - mb) / den);
      xn[2] = rbf(rbf(bflo(xr[1]) - mb) / den); xn[3] = rbf(rbf(bfhi(xr[1]) - mb) / den);
      *(f32x4*)(smem + L_XN + w * 1024 + l * 16) = xn;
    };
    auto fma8 = [&](const u32x4& wv, float xv) {
#pragma unroll
      for (int j = 0; j < 4; ++j) { acc[2 * j] = fmaf(xv, bflo(wv[j]), acc[2 * j]); acc[2 * j + 1] = fmaf(xv, bfhi(wv[j]), acc[2 * j + 1]); }
    };
    u32x4 T[32];
#pragma unroll
    for (int i = 0; i < 8; ++i) T[i] = tload(i);
    SBAR();
    E = __hip_atomic_load((gu64*)(p.ws + WS_EPOCH), RLX_AGENT);
    SBAR();
    if (isq) {
#pragma unroll
      for (int i = 8; i < 32; ++i) T[i] = tload(i);
      SBAR();
      issueK(A, s); issueV(A, s); issueK(B, s + 64); issueV(B, s + 64);
    }
    asm volatile("" ::: "memory");
    SBAR();
    lnorm();
    if (isq) {
#pragma unroll
      for (int ib = 0; ib < 8; ++ib) {
        const f32x4 xv = *(const f32x4*)(smem + L_XN + w * 1024 + (32 * ib + 4 * (l >> 3)) * 4);
#pragma unroll
        for (int u = 0; u < 4; ++u) fma8(T[4 * ib + u], xv[u]);
      }
    } else {
#pragma unroll
      for (int ib = 0; ib < 8; ++ib) {
        const f32x4 xv = *(const f32x4*)(smem + L_XN + w * 1024 + (32 * ib + 4 * (l >> 3)) * 4);
#pragma unroll
        for (int u = 0; u < 4; ++u) {
          const int i = 4 * ib + u;
          fma8(T[i & 7], xv[u]);
          SBAR();
          if (i + 8 < 32) T[i & 7] = tload(i + 8);
          SBAR();
        }
        if (ib == 5) issueK(A, s);
        if (ib == 6) issueV(A, s);
        if (ib == 7) { issueK(B, s + 64); issueV(B, s + 64); }
      }
    }
    const float r = reduce8(acc, l);
    const int col = 8 * (l & 7) + 4 * (l >> 5) + 2 * ((l >> 4) & 1) + ((l >> 3) & 1);
    {
      const u64 tg = E + 1;
      tagl = (u32)tg << 24; tagh = (u32)(tg >> 8);
    }
    if (isq) {
      *(float*)(smem + L_QP + (w * 64 + col) * 4) = r;
      wg_barrier();
      if (w == 0) {
        const float* qp = (const float*)(smem + L_QP);
        const float qv = rbf((qp[l] + qp[64 + l]) + (qp[128 + l] + qp[192 + l]));
        const u32x2 gr = {(__builtin_bit_cast(u32, qv) >> 8) | tagl, tagh};
        __builtin_amdgcn_raw_buffer_store_b64(gr, rs, (int)(WS_Q + (g * 1024 + s * 64 + l) * 8), 0, 16);
      }
    } else {
      const u32x2 gr = {enc24(r) | tagl, tagh};
      __builtin_amdgcn_raw_buffer_store_b64(gr, rs, (int)(WS_GU + ((mat * 4 + ks) * FF + cgp * 256 + w * 64 + col) * 8), 0, 16);
    }
  } else {
    issueK(A, s); issueV(A, s); issueK(B, s + 64); issueV(B, s + 64);
    E = __hip_atomic_load((gu64*)(p.ws + WS_EPOCH), RLX_AGENT);
    const u64 tg = E + 1;
    tagl = (u32)tg << 24; tagh = (u32)(tg >> 8);
  }
  STAMP(1);
  auto bad16 = [&](const u32x4& x) -> u32 { return (x[1] ^ tagh) | (x[3] ^ tagh) | (((x[0] ^ tagl) | (x[2] ^ tagl)) >> 24); };

  {
    const int off = (int)(WS_Q + (g * 1024 + w * 256 + 2 * l) * 8);
    u32x4 q0, q1;
    for (u32 spin = 0; spin < SPIN_CAP; ++spin) {
      q0 = __builtin_amdgcn_raw_buffer_load_b128(rs, off, 0, 16);
      q1 = __builtin_amdgcn_raw_buffer_load_b128(rs, off + 1024, 0, 16);
      if (!__any(bad16(q0) | bad16(q1))) break;
      __builtin_amdgcn_s_sleep(2);
    }
    u32* qs = (u32*)(smem + L_QS);
    qs[w * 128 + l] = ((q0[0] >> 8) & 0xffffu) | ((q0[2] << 8) & 0xffff0000u);
    qs[w * 128 + 64 + l] = ((q1[0] >> 8) & 0xffffu) | ((q1[2] << 8) & 0xffff0000u);
  }
  wg_barrier();
  STAMP(2);
  u32x4 qf[2];
#pragma unroll
  for (int s2 = 0; s2 < 2; ++s2) qf[s2] = *(const u32x4*)(smem + L_QS + ((l & 7) * 128 + (8 * s2 + 2 * g4 + c2) * 8) * 2);

  float m = -3.0e38f, lsum = 0.f;
  f32x4 acc[8];
#pragma unroll
  for (int j = 0; j < 8; ++j) acc[j] = f32x4{0.f, 0.f, 0.f, 0.f};
  u32x2 pa;
  auto qk = [&](const Unit& U, bool valid) {
    const f32x4 z = {0.f, 0.f, 0.f, 0.f};
    f32x4 d0 = mfma32(U.k[0], qf[0], z); d0 = mfma32(U.k[1], qf[1], d0);
    f32x4 d1 = mfma32(U.k[2], qf[0], z); d1 = mfma32(U.k[3], qf[1], d1);
    float t0 = (c2 ? d0[3] : d0[0]) + dpp<XOR8>(c2 ? d0[1] : d0[2]);
    float t1 = (c2 ? d1[3] : d1[0]) + dpp<XOR8>(c2 ? d1[1] : d1[2]);
    t0 = rbf(rbf(t0) * 0.08838834764831845f) * 1.4426950408889634f;
    t1 = rbf(rbf(t1) * 0.08838834764831845f) * 1.4426950408889634f;
    if (!valid) { t0 = -3.0e38f; t1 = -3.0e38f; }
    const float mx = fmaxf(t0, t1);
    if (__any(mx > m)) {
      float mn = fmaxf(mx, dpp<XOR8>(mx));
      mn = fmaxf(max32(max16(mn)), m);
      const float al = __builtin_amdgcn_exp2f(m - mn);
      m = mn; lsum *= al;
      float ar[4];
#pragma unroll
      for (int r = 0; r < 4; ++r) {
        const float lo = __builtin_bit_cast(float, __builtin_amdgcn_readlane(__builtin_bit_cast(int, al), r));
        const float hi = __builtin_bit_cast(float, __builtin_amdgcn_readlane(__builtin_bit_cast(int, al), 4 + r));
        ar[r] = (g4 & 1) ? hi : lo;
      }
#pragma unroll
      for (int j = 0; j < 8; ++j)
#pragma unroll
        for (int r = 0; r < 4; ++r) acc[j][r] *= ar[r];
    }
    const float p0 = __builtin_amdgcn_exp2f(t0 - m), p1 = __builtin_amdgcn_exp2f(t1 - m);
    lsum += p0 + p1;
    const u32 pk = pkbf(p0, p1);
    pa[0] = c2 ? 0u : pk; pa[1] = c2 ? pk : 0u;
  };
  auto pv = [&](const Unit& U) {
#pragma unroll
    for (int j = 0; j < 8; ++j) {
      const u32 sel = (j & 1) ? 0x07060302u : 0x05040100u;
      u32x2 f;
      f[0] = __builtin_amdgcn_perm(U.v[2][j >> 1], U.v[0][j >> 1], sel);
      f[1] = __builtin_amdgcn_perm(U.v[3][j >> 1], U.v[1][j >> 1], sel);
      acc[j] = mfma16(pa, f, acc[j]);
    }
  };
  const int rgl = s >> 2, cg = s & 3, RG = g * 16 + rgl;
  u32x4 Fd[7], Fo[8];
  auto issueF = [&]() {
    const unsigned char* db = (const unsigned char*)(p.wd + (size_t)(56 * RG) * DM + 256 * cg + 64 * w) + (u32)(7 * (l >> 3)) * 2048u + (u32)(l & 7) * 16u;
#pragma unroll
    for (int i = 0; i < 7; ++i) Fd[i] = ldnt(db + i * 2048);
    const unsigned char* ob = (const unsigned char*)(p.wo + (size_t)(64 * RG) * DM + 256 * cg + 64 * w) + (u32)(4 * (l >> 3)) * 2048u + (u32)(l & 7) * 16u;
#pragma unroll
    for (int i = 0; i < 8; ++i) Fo[i] = ldnt(ob + (32 * (i >> 2) + (i & 3)) * 2048);
    SBAR();
  };
  u32x2 gx[8];
  const int fidx = 56 * RG + (l < 56 ? l : 0);
  auto issue_gu = [&]() {
#pragma unroll
    for (int i = 0; i < 8; ++i) gx[i] = __builtin_amdgcn_raw_buffer_load_b64(rs, (int)(WS_GU + (i * FF + fidx) * 8), 0, 16);
    SBAR();
  };
  auto compute_h = [&]() {
    auto gu_bad = [&]() -> bool {
      u32 bad = 0;
#pragma unroll
      for (int i = 0; i < 8; ++i) bad |= (gx[i][1] ^ tagh) | ((gx[i][0] ^ tagl) >> 24);
      return __any(bad);
    };
    if (gu_bad()) {
      for (u32 spin = 0; spin < SPIN_CAP; ++spin) {
        __builtin_amdgcn_s_sleep(2);
        issue_gu();
        if (!gu_bad()) break;
      }
    }
    float a[8];
#pragma unroll
    for (int i = 0; i < 8; ++i) a[i] = __builtin_bit_cast(float, gx[i][0] << 8);
    const float gt = rbf((a[0] + a[1]) + (a[2] + a[3])), up = rbf((a[4] + a[5]) + (a[6] + a[7]));
    const float ge = rbf(0.5f * gt * (1.f + tanhf(0.7978845608028654f * (gt + 0.044715f * gt * gt * gt))));
    if (l < 56) *(float*)(smem + L_H + ((l / 7) * 8 + (l % 7)) * 4) = rbf(ge * up);
  };
  gu32* cnt = (gu32*)(p.ws + WS_CNT + (g * 4 + w) * 256);
  auto ticket = [&](int k) -> u32 {
    return (u32)(64 * k + s);
  };
#define TICKET_GET(v, N) (v)
  {
    u32 nA = s + 128, nB = s + 192;
    int kk = 0;
    u32 vA = ticket(kk++), vB = ticket(kk++);
    SBAR();
    bool bvalid = true;
    bool first = true;
    do {
      qk(A, true);
      issueK(A, nA);
      pv(A);
      issueV(A, nA);
      first = false;
      nA = PSTAT * 64 + TICKET_GET(vA, 16);
      vA = ticket(kk++);
      SBAR();
      qk(B, true);
      issueK(B, nB);
      pv(B);
      issueV(B, nB);
      bvalid = nB < (u32)NUNIT_POOL;
      nB = PSTAT * 64 + TICKET_GET(vB, 16);
      vB = ticket(kk++);
      SBAR();
    } while (nA < (u32)NUNIT_POOL);
    if (w == 3) issue_gu();
    issueF();
    qk(A, true); pv(A);
    qk(B, bvalid); pv(B);
  }
  STAMP(3);

  {
    float ls = lsum + dpp<XOR8>(lsum);
    ls = fold16(ls, ls); ls = fold32(ls, ls);
    if (l < 8) { *(float*)(smem + L_SM + (w * 8 + l) * 4) = m; *(float*)(smem + L_SL + (w * 8 + l) * 4) = ls; }
#pragma unroll
    for (int r = 0; r < 4; ++r) {
      f32x4 o;
#pragma unroll
      for (int j = 0; j < 4; ++j) o[j] = fold32(acc[j][r], acc[j + 4][r]);
      *(f32x4*)(smem + L_SO + ((w * 8 + 4 * (g4 & 1) + r) * 128 + 8 * (l & 15) + 4 * (l >> 5)) * 4) = o;
    }
  }
  wg_barrier();
  STAMP(4);
  {
    const int hd = 2 * w + (l >> 5), e = l & 31;
    const float* sm = (const float*)(smem + L_SM);
    const float* sl = (const float*)(smem + L_SL);
    const float m0 = sm[hd], m1 = sm[8 + hd], m2 = sm[16 + hd], m3 = sm[24 + hd];
    const float M = fmaxf(fmaxf(m0, m1), fmaxf(m2, m3));
    const float w0 = __builtin_amdgcn_exp2f(m0 - M), w1 = __builtin_amdgcn_exp2f(m1 - M), w2 = __builtin_amdgcn_exp2f(m2 - M), w3 = __builtin_amdgcn_exp2f(m3 - M);
    const float L = (w0 * sl[hd] + w1 * sl[8 + hd]) + (w2 * sl[16 + hd] + w3 * sl[24 + hd]);
    const float inv = 1.f / L;
    const f32x4* so = (const f32x4*)(smem + L_SO);
    const f32x4 o = (so[(0 * 8 + hd) * 32 + e] * w0 + so[(1 * 8 + hd) * 32 + e] * w1) + (so[(2 * 8 + hd) * 32 + e] * w2 + so[(3 * 8 + hd) * 32 + e] * w3);
    const int off = (int)(WS_O + ((((g * 8 + hd) * 2 + (e >> 4)) * 64 + s) * 64 + 4 * (e & 15)) * 8);
    const u32x4 g0 = {enc24(o[0] * inv) | tagl, tagh, enc24(o[1] * inv) | tagl, tagh};
    const u32x4 g1 = {enc24(o[2] * inv) | tagl, tagh, enc24(o[3] * inv) | tagl, tagh};
    __builtin_amdgcn_raw_buffer_store_b128(g0, rs, off, 0, 16);
    __builtin_amdgcn_raw_buffer_store_b128(g1, rs, off + 16, 0, 16);
    if (e == 0) {
      const float lse = M + __builtin_amdgcn_logf(L);
      const u32 hi = enc24(lse);
      const float lo = lse - __builtin_bit_cast(float, hi << 8);
      const u32x4 gl = {hi | tagl, tagh, enc24(lo) | tagl, tagh};
      __builtin_amdgcn_raw_buffer_store_b128(gl, rs, (int)(WS_LSE + ((g * 8 + hd) * 64 + s) * 16), 0, 16);
    }
  }
  SBAR();
  const u32x2 xfin = *(const u32x2*)(p.x + 4 * b);
  if (w == 3) compute_h();
  wg_barrier();
  float oacc[8];
#pragma unroll
  for (int j = 0; j < 8; ++j) oacc[j] = 0.f;
  {
    const f32x4 h0 = *(const f32x4*)(smem + L_H + (l >> 3) * 32), h1 = *(const f32x4*)(smem + L_H + (l >> 3) * 32 + 16);
#pragma unroll
    for (int i = 0; i < 7; ++i) {
      const float hv = i < 4 ? h0[i & 3] : h1[i & 3];
#pragma unroll
      for (int j = 0; j < 4; ++j) { oacc[2 * j] = fmaf(hv, bflo(Fd[i][j]), oacc[2 * j]); oacc[2 * j + 1] = fmaf(hv, bfhi(Fd[i][j]), oacc[2 * j + 1]); }
    }
  }
  STAMP(5);
  {
    const int hd = rgl >> 1, half = rgl & 1;
    const int offl = (int)(WS_LSE + ((g * 8 + hd) * 64 + l) * 16);
    const int offo = (int)(WS_O + ((((g * 8 + hd) * 2 + half) * 64 + 16 * w + (l >> 5)) * 64 + 2 * (l & 31)) * 8);
    u32x4 xl, xo[8];
    u32 pend = 0x1ffu;
    for (u32 spin = 0; spin < SPIN_CAP; ++spin) {
      if (pend & 0x100u) xl = __builtin_amdgcn_raw_buffer_load_b128(rs, offl, 0, 16);
#pragma unroll
      for (int i = 0; i < 8; ++i)
        if (pend & (1u << i)) xo[i] = __builtin_amdgcn_raw_buffer_load_b128(rs, offo + i * 1024, 0, 16);
      u32 np = __any(bad16(xl)) ? 0x100u : 0u;
#pragma unroll
      for (int i = 0; i < 8; ++i) np |= __any(bad16(xo[i])) ? (1u << i) : 0u;
      pend = np;
      if (!pend) break;
      __builtin_amdgcn_s_sleep(1);
    }
    const float lse = __builtin_bit_cast(float, xl[0] << 8) + __builtin_bit_cast(float, xl[2] << 8);
    const float M = wave_max(lse);
    const float wt = __builtin_amdgcn_exp2f(lse - M);
    const float wn = wt / wave_sum(wt);
    float o0 = 0.f, o1 = 0.f;
#pragma unroll
    for (int i = 0; i < 8; ++i) {
      const float wa = __builtin_bit_cast(float, __builtin_amdgcn_readlane(__builtin_bit_cast(int, wn), 16 * w + 2 * i));
      const float wb = __builtin_bit_cast(float, __builtin_amdgcn_readlane(__builtin_bit_cast(int, wn), 16 * w + 2 * i + 1));
      const float ws_ = (l >> 5) ? wb : wa;
      o0 = fmaf(ws_, __builtin_bit_cast(float, xo[i][0] << 8), o0);
      o1 = fmaf(ws_, __builtin_bit_cast(float, xo[i][2] << 8), o1);
    }
    *(float*)(smem + L_OP + (w * 64 + 2 * (l & 31) + (l >> 5)) * 4) = fold32(o0, o1);
  }
  wg_barrier();
  STAMP(6);
  {
    const f32x4* op = (const f32x4*)(smem + L_OP);
#pragma unroll
    for (int ib = 0; ib < 2; ++ib) {
      const int idx = 8 * ib + (l >> 3);
      const f32x4 ov = (op[idx] + op[16 + idx]) + (op[32 + idx] + op[48 + idx]);
#pragma unroll
      for (int u = 0; u < 4; ++u) {
        const float o = rbf(ov[u]);
        const u32x4 wv = Fo[4 * ib + u];
#pragma unroll
        for (int j = 0; j < 4; ++j) { oacc[2 * j] = fmaf(o, bflo(wv[j]), oacc[2 * j]); oacc[2 * j + 1] = fmaf(o, bfhi(wv[j]), oacc[2 * j + 1]); }
      }
    }
    const float r = reduce8(oacc, l);
    const int col = 256 * cg + 64 * w + 8 * (l & 7) + 4 * (l >> 5) + 2 * ((l >> 4) & 1) + ((l >> 3) & 1);
    const u32x2 gr = {enc24(r) | tagl, tagh};
    __builtin_amdgcn_raw_buffer_store_b64(gr, rs, (int)(WS_IN + (((col >> 2) * 64 + RG) * 4 + (col & 3)) * 8), 0, 16);
  }
  STAMP(7);
  if (w == 0) {
    const int off = (int)(WS_IN + (b * 64 + l) * 32);
    u32x4 y0, y1;
    for (u32 spin = 0; spin < SPIN_CAP; ++spin) {
      y0 = __builtin_amdgcn_raw_buffer_load_b128(rs, off, 0, 16);
      y1 = __builtin_amdgcn_raw_buffer_load_b128(rs, off + 16, 0, 16);
      if (!__any(bad16(y0) | bad16(y1))) break;
      __builtin_amdgcn_s_sleep(1);
    }
    const float v0 = __builtin_bit_cast(float, y0[0] << 8), v1 = __builtin_bit_cast(float, y0[2] << 8);
    const float v2 = __builtin_bit_cast(float, y1[0] << 8), v3 = __builtin_bit_cast(float, y1[2] << 8);
    const float a0 = fold32(v0, v2), a1 = fold32(v1, v3);
    float r = fold16(a0, a1);
    r += dpp<XOR1>(r); r += dpp<XOR2>(r); r += dpp<XOR7>(r); r += dpp<XOR8>(r);
    if ((l & 15) == 0) {
      const u32 xw = (g4 & 2) ? xfin[1] : xfin[0];
      const float xv = (g4 & 1) ? bfhi(xw) : bflo(xw);
      p.out[4 * b + g4] = (u16)f2bf(xv + r);
    }
    if (b == 0 && l == 0) __hip_atomic_store((gu64*)(p.ws + WS_EPOCH), E + 1, RLX_AGENT);
  }
  STAMP(8);
}

extern "C" void kernel_launch(void* const* d_in, const int* in_sizes, int n_in, void* d_out, int out_size, void* d_ws, size_t ws_size,
                              hipStream_t stream) {
  Params p;
  p.x = (const u16*)d_in[0];
  p.K = (const u16*)d_in[1];
  p.V = (const u16*)d_in[2];
  p.wq = (const u16*)d_in[3];
  p.wo = (const u16*)d_in[4];
  p.wg = (const u16*)d_in[6];
  p.wu = (const u16*)d_in[7];
  p.wd = (const u16*)d_in[8];
  p.out = (u16*)d_out;
  p.ws = (unsigned char*)d_ws;
  fused_decode<<<NWG, NT, 0, stream>>>(p);
}
```

```cpp
#include <hip/hip_runtime.h>
#include <hip/hip_bf16.h>
#include <stdint.h>

typedef unsigned u32;
typedef unsigned long long u64;
typedef unsigned short u16;
typedef u32 u32x4 __attribute__((ext_vector_type(4)));
typedef u32 u32x2 __attribute__((ext_vector_type(2)));
typedef float f32x4 __attribute__((ext_vector_type(4)));
typedef float f32x2 __attribute__((ext_vector_type(2)));
typedef short bf16x8 __attribute__((ext_vector_type(8)));
typedef short bf16x4 __attribute__((ext_vector_type(4)));
#define GAS __attribute__((address_space(1)))
typedef GAS u64 gu64;
#define RLX_AGENT __ATOMIC_RELAXED, __HIP_MEMORY_SCOPE_AGENT

constexpr int DM = 1024, SK = 65536, DH = 128, FF = 3584;
constexpr int NWG = 256, NW = 8, NT = NW * 64;
constexpr u32 WS_EPOCH = 0;
constexpr u32 WS_Q = 16384;
constexpr u32 WS_GU = WS_Q + 4 * 1024 * 8;
constexpr u32 WS_LSE = WS_GU + 2 * 4 * FF * 8;
constexpr u32 WS_O = WS_LSE + 4 * 8 * 64 * 16;
constexpr u32 WS_IN = WS_O + 4 * 8 * 16 * 64 * 8 * 8;
constexpr u32 WS_END = WS_IN + 256 * 128 * 4 * 8;
constexpr u32 WS_DBG = WS_END;
constexpr u32 SPIN_CAP = 1u << 17;
constexpr u32 OOB = 0x40000000u;
constexpr int L_XN = 0;
constexpr int L_QS = L_XN + 2048;
constexpr int L_PT = L_QS + 2048;
constexpr int L_SM = L_PT + 4096;
constexpr int L_SL = L_SM + 256;
constexpr int L_OB = L_SL + 256;
constexpr int L_HB = L_OB + 128;
constexpr int L_SO = L_HB + 128;
constexpr int L_BYTES = 88 * 1024;

struct Params {
  const u16* x; const u16* K; const u16* V; const u16* wq; const u16* wo; const u16* wg; const u16* wu; const u16* wd;
  u16* out; unsigned char* ws;
};

__device__ __forceinline__ float asf(u32 u) { return __builtin_bit_cast(float, u); }
__device__ __forceinline__ u32 asu(float f) { return __builtin_bit_cast(u32, f); }
__device__ __forceinline__ float bflo(u32 u) { return asf(u << 16); }
__device__ __forceinline__ float bfhi(u32 u) { return asf(u & 0xffff0000u); }
__device__ __forceinline__ float rbf(float f) { return (float)(__bf16)f; }
__device__ __forceinline__ u32 f2bf(float f) { return (u32)__builtin_bit_cast(u16, (__bf16)f); }
__device__ __forceinline__ u32 pkbf(float lo, float hi) {
  typedef __bf16 bf2 __attribute__((ext_vector_type(2)));
  f32x2 v = {lo, hi};
  return __builtin_bit_cast(u32, __builtin_convertvector(v, bf2));
}
__device__ __forceinline__ f32x4 mfma32(u32x4 a, u32x4 b, f32x4 c) {
  return __builtin_amdgcn_mfma_f32_16x16x32_bf16(__builtin_bit_cast(bf16x8, a), __builtin_bit_cast(bf16x8, b), c, 0, 0, 0);
}
__device__ __forceinline__ f32x4 mfma16(u32x2 a, u32x2 b, f32x4 c) {
  return __builtin_amdgcn_mfma_f32_16x16x16bf16_1k(__builtin_bit_cast(bf16x4, a), __builtin_bit_cast(bf16x4, b), c, 0, 0, 0);
}
template <int CTRL> __device__ __forceinline__ float dpp(float x) {
  return asf((u32)__builtin_amdgcn_mov_dpp(__builtin_bit_cast(int, x), CTRL, 0xf, 0xf, true));
}
constexpr int XOR1 = 0xB1, XOR2 = 0x4E, XOR7 = 0x141, XOR8 = 0x128, ROR4 = 0x124;
__device__ __forceinline__ float fold32(float a, float b) {
  auto r = __builtin_amdgcn_permlane32_swap(asu(a), asu(b), false, false);
  return asf((u32)r[0]) + asf((u32)r[1]);
}
__device__ __forceinline__ float fold16(float a, float b) {
  auto r = __builtin_amdgcn_permlane16_swap(asu(a), asu(b), false, false);
  return asf((u32)r[0]) + asf((u32)r[1]);
}
__device__ __forceinline__ float max32(float a) {
  auto r = __builtin_amdgcn_permlane32_swap(asu(a), asu(a), false, false);
  return fmaxf(asf((u32)r[0]), asf((u32)r[1]));
}
__device__ __forceinline__ float max16(float a) {
  auto r = __builtin_amdgcn_permlane16_swap(asu(a), asu(a), false, false);
  return fmaxf(asf((u32)r[0]), asf((u32)r[1]));
}
__device__ __forceinline__ float wave_sum(float v) {
  v += dpp<XOR1>(v); v += dpp<XOR2>(v); v += dpp<XOR7>(v); v += dpp<XOR8>(v);
  v = fold16(v, v); v = fold32(v, v);
  return v;
}
__device__ __forceinline__ float wave_max(float v) {
  v = fmaxf(v, dpp<XOR1>(v)); v = fmaxf(v, dpp<XOR2>(v)); v = fmaxf(v, dpp<XOR7>(v)); v = fmaxf(v, dpp<XOR8>(v));
  return max32(max16(v));
}
__device__ __forceinline__ u32x4 ldnt(const unsigned char* p) { return __builtin_nontemporal_load((const u32x4*)p); }
#define SBAR() __builtin_amdgcn_sched_barrier(0)
__device__ __forceinline__ void wg_barrier() {
  asm volatile("s_waitcnt lgkmcnt(0)" ::: "memory");
  __builtin_amdgcn_s_barrier();
  asm volatile("" ::: "memory");
}
__device__ __forceinline__ u32 enc24(float v) { return (asu(v) + 0x80u) >> 8; }

__device__ __forceinline__ void transpose8(const u32x4 (&T)[8], u32x4 (&Bf)[8]) {
#pragma unroll
  for (int j = 0; j < 8; ++j) {
    const u32 sel = (j & 1) ? 0x07060302u : 0x05040100u;
#pragma unroll
    for (int u = 0; u < 4; ++u) Bf[j][u] = __builtin_amdgcn_perm(T[2 * u + 1][j >> 1], T[2 * u][j >> 1], sel);
  }
}

#define STAMP(i) do { } while (0)

struct Unit { u32x4 k[4]; u32x4 v[4]; };

__global__ void __launch_bounds__(NT, 2) fused_decode(Params p) {
  __shared__ __attribute__((aligned(16))) unsigned char smem[L_BYTES];
  const int tid = threadIdx.x, l = tid & 63, w = __builtin_amdgcn_readfirstlane(tid >> 6);
  const int b = blockIdx.x, g = b >> 6, s = b & 63;
  const bool isq = s < 16;
  const __amdgpu_buffer_rsrc_t rs = __builtin_amdgcn_make_buffer_rsrc((void*)p.ws, 0, (int)WS_END, 0x00020000);
  const __amdgpu_buffer_rsrc_t rk = __builtin_amdgcn_make_buffer_rsrc((void*)(p.K + (size_t)g * SK * DH), 0, SK * DH * 2, 0x00020000);
  const __amdgpu_buffer_rsrc_t rv = __builtin_amdgcn_make_buffer_rsrc((void*)(p.V + (size_t)g * SK * DH), 0, SK * DH * 2, 0x00020000);
  STAMP(0);
  const int g4 = l >> 4, c2 = (l >> 3) & 1;
  const u32 koff = (u32)(((l & 15) >> 1) * 256 + (2 * g4 + (l & 1)) * 16);
  const u32 voff = (u32)(g4 * 512 + (l & 15) * 16);
  auto unit_off = [&](int k) -> u32 {
    const int tau = k + (isq ? 2 : 0);
    const int u = tau < 2 ? tau * 384 + (s - 16) * 8 + w : (tau < 8 ? 768 + (tau - 2) * 512 + s * 8 + w : 3840 + s * 4 + w);
    return (tau < 8 || (tau == 8 && w < 4)) ? (u32)u * 4096u : OOB;
  };
  auto issueK = [&](Unit& U, u32 uo) {
    SBAR();
    const int o = (int)(koff + uo);
    U.k[0] = __builtin_amdgcn_raw_buffer_load_b128(rk, o, 0, 2); U.k[1] = __builtin_amdgcn_raw_buffer_load_b128(rk, o + 128, 0, 2);
    U.k[2] = __builtin_amdgcn_raw_buffer_load_b128(rk, o + 2048, 0, 2); U.k[3] = __builtin_amdgcn_raw_buffer_load_b128(rk, o + 2048 + 128, 0, 2);
    SBAR();
  };
  auto issueV = [&](Unit& U, u32 uo) {
    SBAR();
    const int o = (int)(voff + uo);
    U.v[0] = __builtin_amdgcn_raw_buffer_load_b128(rv, o, 0, 2); U.v[1] = __builtin_amdgcn_raw_buffer_load_b128(rv, o + 256, 0, 2);
    U.v[2] = __builtin_amdgcn_raw_buffer_load_b128(rv, o + 2048, 0, 2); U.v[3] = __builtin_amdgcn_raw_buffer_load_b128(rv, o + 2048 + 256, 0, 2);
    SBAR();
  };
  Unit A, B;

  const u32 arow = (u32)(8 * (2 * g4 + (l & 1))) * 2u;
  const f32x4 zero4 = {0.f, 0.f, 0.f, 0.f};
  u64 E;
  u32 tagl, tagh;
  auto lnorm = [&](const u32x4& xa, const u32x4& xb, u32 xr) {
    float xs[16];
#pragma unroll
    for (int j = 0; j < 4; ++j) { xs[2 * j] = bflo(xa[j]); xs[2 * j + 1] = bfhi(xa[j]); xs[8 + 2 * j] = bflo(xb[j]); xs[9 + 2 * j] = bfhi(xb[j]); }
    float sm = 0.f;
#pragma unroll
    for (int j = 0; j < 16; ++j) sm += xs[j];
    const float mean = wave_sum(sm) * (1.f / DM);
    float sv = 0.f;
#pragma unroll
    for (int j = 0; j < 16; ++j) { const float d = xs[j] - mean; sv += d * d; }
    const float var = wave_sum(sv) * (1.f / DM);
    const float mb = rbf(mean), den = rbf(sqrtf(rbf(rbf(var) + 1e-5f)));
    *(u32*)(smem + L_XN + (64 * w + l) * 4) = pkbf(rbf(bflo(xr) - mb) / den, rbf(bfhi(xr) - mb) / den);
  };
  if (isq) {
    const u32x4 xa = *(const u32x4*)(p.x + 16 * l), xb = *(const u32x4*)(p.x + 16 * l + 8);
    const u32 xr = *(const u32*)(p.x + 128 * w + 2 * l);
    SBAR();
    u32x4 T0[8], T1[8];
    const unsigned char* wb = (const unsigned char*)(p.wq + (size_t)(128 * w) * 4096 + g * 1024 + s * 64) + (u32)(8 * (l >> 3)) * 8192u + (u32)(l & 7) * 16u;
#pragma unroll
    for (int i = 0; i < 8; ++i) T0[i] = ldnt(wb + (size_t)i * 8192);
    E = __hip_atomic_load((gu64*)(p.ws + WS_EPOCH), RLX_AGENT);
#pragma unroll
    for (int i = 0; i < 8; ++i) T1[i] = ldnt(wb + (size_t)(64 + i) * 8192);
    SBAR();
    issueK(A, unit_off(0)); issueV(A, unit_off(0));
    asm volatile("; q path" ::: "memory");
    lnorm(xa, xb, xr);
    const u32x4 a0 = *(const u32x4*)(smem + L_XN + 256 * w + arow), a1 = *(const u32x4*)(smem + L_XN + 256 * w + 128 + arow);
    f32x4 d[8];
    u32x4 Bf[8];
    transpose8(T0, Bf);
#pragma unroll
    for (int j = 0; j < 8; ++j) d[j] = mfma32(a0, Bf[j], zero4);
    transpose8(T1, Bf);
#pragma unroll
    for (int j = 0; j < 8; ++j) d[j] = mfma32(a1, Bf[j], d[j]);
    issueK(B, unit_off(1));
    if (l < 16) {
      f32x4 lo, hi;
#pragma unroll
      for (int j = 0; j < 4; ++j) { lo[j] = c2 ? d[j][1] : d[j][0]; hi[j] = c2 ? d[j + 4][1] : d[j + 4][0]; }
      *(f32x4*)(smem + L_PT + ((w * 2 + c2) * 64 + 8 * (l & 7)) * 4) = lo;
      *(f32x4*)(smem + L_PT + ((w * 2 + c2) * 64 + 8 * (l & 7) + 4) * 4) = hi;
    }
    tagl = (u32)(E + 1) << 24; tagh = (u32)((E + 1) >> 8);
    wg_barrier();
    if (w == 0) {
      const float* pt = (const float*)(smem + L_PT);
      float a[16];
#pragma unroll
      for (int i = 0; i < 16; ++i) a[i] = pt[i * 64 + l];
#pragma unroll
      for (int st = 8; st > 0; st >>= 1)
#pragma unroll
        for (int i = 0; i < st; ++i) a[i] += a[i + st];
      const u32x2 gr = {(asu(rbf(a[0])) >> 8) | tagl, tagh};
      __builtin_amdgcn_raw_buffer_store_b64(gr, rs, (int)(WS_Q + (g * 1024 + s * 64 + l) * 8), 0, 16);
    }
  } else {
    issueK(A, unit_off(0));
    const u32x4 xa = *(const u32x4*)(p.x + 16 * l), xb = *(const u32x4*)(p.x + 16 * l + 8);
    const u32 xr = *(const u32*)(p.x + 128 * w + 2 * l);
    E = __hip_atomic_load((gu64*)(p.ws + WS_EPOCH), RLX_AGENT);
    issueV(A, unit_off(0)); issueK(B, unit_off(1));
    asm volatile("; non-q path" ::: "memory");
    lnorm(xa, xb, xr);
    tagl = (u32)(E + 1) << 24; tagh = (u32)((E + 1) >> 8);
  }
  STAMP(1);
  auto bad16 = [&](const u32x4& x) -> u32 { return (x[1] ^ tagh) | (x[3] ^ tagh) | (((x[0] ^ tagl) | (x[2] ^ tagl)) >> 24); };

  {
    const int off = (int)(WS_Q + (g * 1024 + w * 128 + 2 * l) * 8);
    asm volatile("s_waitcnt vmcnt(4)" ::: "memory");
    SBAR();
    u32x4 q0 = __builtin_amdgcn_raw_buffer_load_b128(rs, off, 0, 16);
    issueV(B, unit_off(1));
    if (__any(bad16(q0))) {
      for (u32 spin = 0; spin < SPIN_CAP; ++spin) {
      asm volatile("" ::: "memory");
        q0 = __builtin_amdgcn_raw_buffer_load_b128(rs, off, 0, 16);
        if (!__any(bad16(q0))) break;
        __builtin_amdgcn_s_sleep(1);
      }
    }
    *(u32*)(smem + L_QS + (w * 64 + l) * 4) = ((q0[0] >> 8) & 0xffffu) | ((q0[2] << 8) & 0xffff0000u);
  }
  wg_barrier();
  STAMP(2);
  u32x4 qf[2];
#pragma unroll
  for (int s2 = 0; s2 < 2; ++s2) qf[s2] = *(const u32x4*)(smem + L_QS + ((l & 7) * 128 + (8 * s2 + 2 * g4 + c2) * 8) * 2);

  float m = -3.0e38f, lsum = 0.f;
  f32x4 acc[8];
#pragma unroll
  for (int j = 0; j < 8; ++j) acc[j] = zero4;
  u32x2 pa;
  auto qk = [&](const Unit& U, bool valid) {
    f32x4 d0 = mfma32(U.k[0], qf[0], zero4); d0 = mfma32(U.k[1], qf[1], d0);
    f32x4 d1 = mfma32(U.k[2], qf[0], zero4); d1 = mfma32(U.k[3], qf[1], d1);
    float t0 = (c2 ? d0[3] : d0[0]) + dpp<XOR8>(c2 ? d0[1] : d0[2]);
    float t1 = (c2 ? d1[3] : d1[0]) + dpp<XOR8>(c2 ? d1[1] : d1[2]);
    t0 = rbf(rbf(t0) * 0.08838834764831845f) * 1.4426950408889634f;
    t1 = rbf(rbf(t1) * 0.08838834764831845f) * 1.4426950408889634f;
    if (!valid) { t0 = -3.0e38f; t1 = -3.0e38f; }
    const float mx = fmaxf(t0, t1);
    if (__any(mx > m)) {
      float mn = fmaxf(mx, dpp<XOR8>(mx));
      mn = fmaxf(max32(max16(mn)), m);
      const float al = __builtin_amdgcn_exp2f(m - mn);
      m = mn; lsum *= al;
      float ar[4];
#pragma unroll
      for (int r = 0; r < 4; ++r) {
        const float lo = asf((u32)__builtin_amdgcn_readlane((int)asu(al), r)), hi = asf((u32)__builtin_amdgcn_readlane((int)asu(al), 4 + r));
        ar[r] = (g4 & 1) ? hi : lo;
      }
#pragma unroll
      for (int j = 0; j < 8; ++j)
#pragma unroll
        for (int r = 0; r < 4; ++r) acc[j][r] *= ar[r];
    }
    const float p0 = __builtin_amdgcn_exp2f(t0 - m), p1 = __builtin_amdgcn_exp2f(t1 - m);
    lsum += p0 + p1;
    const u32 pk = pkbf(p0, p1);
    pa[0] = c2 ? 0u : pk; pa[1] = c2 ? pk : 0u;
  };
  auto pv = [&](const Unit& U) {
#pragma unroll
    for (int j = 0; j < 8; ++j) {
      const u32 sel = (j & 1) ? 0x07060302u : 0x05040100u;
      u32x2 f;
      f[0] = __builtin_amdgcn_perm(U.v[2][j >> 1], U.v[0][j >> 1], sel);
      f[1] = __builtin_amdgcn_perm(U.v[3][j >> 1], U.v[1][j >> 1], sel);
      acc[j] = mfma16(pa, f, acc[j]);
    }
  };
  const bool hasgu = s >= 8;
  const int tgu = g * 56 + (hasgu ? s - 8 : 0), mat = tgu / 112, tt = tgu % 112, cg2 = tt >> 2, ks = tt & 3;
  const int rgl = s >> 2, cg = s & 3, RG = g * 16 + rgl;
  u32x4 G[8], F[8];
  {
    const int nun = (isq ? 6 : 8) + (w < 4 ? 1 : 0), npair = (nun + 1) >> 1;
    for (int i = 0; i + 1 < npair; ++i) {
      const u32 ua = unit_off(2 * i + 2), ub = unit_off(2 * i + 3);
      qk(A, true); issueK(A, ua); pv(A); issueV(A, ua);
      qk(B, true); issueK(B, ub); pv(B); issueV(B, ub);
    }
    qk(A, true);
    {
      SBAR();
      const __amdgpu_buffer_rsrc_t rg = __builtin_amdgcn_make_buffer_rsrc((void*)(mat ? p.wu : p.wg), 0, DM * FF * 2, 0x00020000);
      const int o = hasgu ? (int)((u32)(256 * ks + 64 * (w >> 1) + 8 * (l >> 3)) * 7168u + (u32)(cg2 * 128 + (w & 1) * 64 + (l & 7) * 8) * 2u) : (int)OOB;
#pragma unroll
      for (int i = 0; i < 8; ++i) G[i] = __builtin_amdgcn_raw_buffer_load_b128(rg, o, i * 7168, 2);
      SBAR();
    }
    pv(A);
    qk(B, (nun & 1) == 0);
    {
      SBAR();
      const bool dn = w >= 4;
      const __amdgpu_buffer_rsrc_t rf = __builtin_amdgcn_make_buffer_rsrc((void*)(dn ? p.wd : p.wo), 0, dn ? FF * DM * 2 : 4096 * DM * 2, 0x00020000);
      u32 o = (u32)((dn ? 56 : 64) * RG + 8 * (l >> 3)) * 2048u + (u32)(256 * cg + 64 * (w & 3) + (l & 7) * 8) * 2u;
      if (dn && l >= 56) o = OOB;
#pragma unroll
      for (int i = 0; i < 8; ++i) F[i] = __builtin_amdgcn_raw_buffer_load_b128(rf, (int)o, i * 2048, 2);
      SBAR();
    }
    pv(B);
  }
  STAMP(3);

  {
    float ls = lsum + dpp<XOR8>(lsum);
    ls = fold16(ls, ls); ls = fold32(ls, ls);
    if (l < 8) { *(float*)(smem + L_SM + (l * 8 + w) * 4) = m; *(float*)(smem + L_SL + (l * 8 + w) * 4) = ls; }
#pragma unroll
    for (int r = 0; r < 4; ++r) {
      f32x4 o;
#pragma unroll
      for (int j = 0; j < 4; ++j) o[j] = fold32(acc[j][r], acc[j + 4][r]);
      *(f32x4*)(smem + L_SO + ((w * 8 + 4 * (g4 & 1) + r) * 128 + 8 * (l & 15) + 4 * (l >> 5)) * 4) = o;
    }
  }
  wg_barrier();
  STAMP(4);
  {
    const f32x4 ma = *(const f32x4*)(smem + L_SM + w * 32), mb = *(const f32x4*)(smem + L_SM + w * 32 + 16);
    const f32x4 la = *(const f32x4*)(smem + L_SL + w * 32), lb = *(const f32x4*)(smem + L_SL + w * 32 + 16);
    const float M = fmaxf(fmaxf(fmaxf(ma[0], ma[1]), fmaxf(ma[2], ma[3])), fmaxf(fmaxf(mb[0], mb[1]), fmaxf(mb[2], mb[3])));
    float L = 0.f, o0 = 0.f, o1 = 0.f;
#pragma unroll
    for (int i = 0; i < 8; ++i) {
      const float wt = __builtin_amdgcn_exp2f((i < 4 ? ma[i & 3] : mb[i & 3]) - M);
      const f32x2 ov = *(const f32x2*)(smem + L_SO + ((i * 8 + w) * 128 + 2 * l) * 4);
      L = fmaf(wt, i < 4 ? la[i & 3] : lb[i & 3], L); o0 = fmaf(wt, ov[0], o0); o1 = fmaf(wt, ov[1], o1);
    }
    const float inv = 1.f / L;
    const u32x4 g0 = {enc24(o0 * inv) | tagl, tagh, enc24(o1 * inv) | tagl, tagh};
    __builtin_amdgcn_raw_buffer_store_b128(g0, rs, (int)(WS_O + ((((g * 8 + w) * 16 + (l >> 2)) * 64 + s) * 8 + 2 * (l & 3)) * 8), 0, 16);
    if (l == 0) {
      const float lse = M + __builtin_amdgcn_logf(L);
      const u32 hi = enc24(lse);
      const u32x4 gl = {hi | tagl, tagh, enc24(lse - asf(hi << 8)) | tagl, tagh};
      __builtin_amdgcn_raw_buffer_store_b128(gl, rs, (int)(WS_LSE + ((g * 8 + w) * 64 + s) * 16), 0, 16);
    }
  }
  SBAR();
  STAMP(5);
  if (hasgu) {
    const u32x4 a0 = *(const u32x4*)(smem + L_XN + (256 * ks + 64 * (w >> 1)) * 2 + arow);
    u32x4 Bf[8];
    transpose8(G, Bf);
    f32x4 d[8];
#pragma unroll
    for (int j = 0; j < 8; ++j) d[j] = mfma32(a0, Bf[j], zero4);
    if (l < 16) {
      f32x4 lo, hi;
#pragma unroll
      for (int j = 0; j < 4; ++j) { lo[j] = c2 ? d[j][1] : d[j][0]; hi[j] = c2 ? d[j + 4][1] : d[j + 4][0]; }
      *(f32x4*)(smem + L_PT + (((w >> 1) * 2 + c2) * 128 + (w & 1) * 64 + 8 * (l & 7)) * 4) = lo;
      *(f32x4*)(smem + L_PT + (((w >> 1) * 2 + c2) * 128 + (w & 1) * 64 + 8 * (l & 7) + 4) * 4) = hi;
    }
    wg_barrier();
    if (w < 2) {
      const float* pt = (const float*)(smem + L_PT) + w * 64 + l;
      const float r = ((pt[0] + pt[128]) + (pt[256] + pt[384])) + ((pt[512] + pt[640]) + (pt[768] + pt[896]));
      const u32x2 gr = {enc24(r) | tagl, tagh};
      __builtin_amdgcn_raw_buffer_store_b64(gr, rs, (int)(WS_GU + ((mat * 4 + ks) * FF + cg2 * 128 + w * 64 + l) * 8), 0, 16);
    }
  }
  STAMP(6);
  const u32x2 xfin = *(const u32x2*)(p.x + 4 * b);
  u32x4 Ff[8];
  transpose8(F, Ff);
#pragma unroll
  for (int j = 0; j < 8; ++j) asm volatile("" : "+v"(Ff[j]));
  {
    const int hd = rgl >> 1, half = rgl & 1;
    const int offl = (int)(WS_LSE + ((g * 8 + hd) * 64 + (l >> 2)) * 16);
    const int offo = (int)(WS_O + (((g * 8 + hd) * 16 + half * 8 + w) * 64 * 8 + 2 * l) * 8);
    u32x4 xl[4], xo[4];
    u32 pend = 0xfu;
    for (u32 spin = 0; spin < SPIN_CAP; ++spin) {
      asm volatile("" ::: "memory");
#pragma unroll
      for (int i = 0; i < 4; ++i)
        if (pend & (1u << i)) {
          xl[i] = __builtin_amdgcn_raw_buffer_load_b128(rs, offl + i * 256, 0, 16);
          xo[i] = __builtin_amdgcn_raw_buffer_load_b128(rs, offo + i * 1024, 0, 16);
        }
      u32 np = 0;
#pragma unroll
      for (int i = 0; i < 4; ++i) np |= __any(bad16(xl[i]) | bad16(xo[i])) ? (1u << i) : 0u;
      pend = np;
      if (!pend) break;
    }
    float lse[4];
#pragma unroll
    for (int i = 0; i < 4; ++i) lse[i] = asf(xl[i][0] << 8) + asf(xl[i][2] << 8);
    float M = fmaxf(fmaxf(lse[0], lse[1]), fmaxf(lse[2], lse[3]));
    M = fmaxf(M, dpp<ROR4>(M)); M = fmaxf(M, dpp<XOR8>(M)); M = max32(max16(M));
    float W = 0.f, o0 = 0.f, o1 = 0.f;
#pragma unroll
    for (int i = 0; i < 4; ++i) {
      const float wt = __builtin_amdgcn_exp2f(lse[i] - M);
      W += wt; o0 = fmaf(wt, asf(xo[i][0] << 8), o0); o1 = fmaf(wt, asf(xo[i][2] << 8), o1);
    }
    float o = fold32(o0, o1);
    W = fold32(W, W);
    o = fold16(o, o); W = fold16(W, W);
    o += dpp<XOR8>(o); W += dpp<XOR8>(W);
    o += dpp<ROR4>(o); W += dpp<ROR4>(W);
    if ((l & 28) == 0) *(u16*)(smem + L_OB + (8 * w + 2 * (l & 3) + (l >> 5)) * 2) = (u16)f2bf(o / W);
  }
  if (w == 7) {
    const int fidx = 56 * RG + (l < 56 ? l : 0);
    u32x2 gx[8];
    for (u32 spin = 0; spin < SPIN_CAP; ++spin) {
      asm volatile("" ::: "memory");
      u32 bad = 0;
#pragma unroll
      for (int i = 0; i < 8; ++i) gx[i] = __builtin_amdgcn_raw_buffer_load_b64(rs, (int)(WS_GU + (i * FF + fidx) * 8), 0, 16);
#pragma unroll
      for (int i = 0; i < 8; ++i) bad |= (gx[i][1] ^ tagh) | ((gx[i][0] ^ tagl) >> 24);
      if (!__any(bad)) break;
    }
    float a[8];
#pragma unroll
    for (int i = 0; i < 8; ++i) a[i] = asf(gx[i][0] << 8);
    const float gt = rbf((a[0] + a[1]) + (a[2] + a[3])), up = rbf((a[4] + a[5]) + (a[6] + a[7]));
    const float ge = rbf(0.5f * gt * (1.f + tanhf(0.7978845608028654f * (gt + 0.044715f * gt * gt * gt))));
    *(u16*)(smem + L_HB + l * 2) = l < 56 ? (u16)f2bf(ge * up) : (u16)0;
  }
  wg_barrier();
  STAMP(7);
  {
    const u32x4 a0 = *(const u32x4*)(smem + (w < 4 ? L_OB : L_HB) + arow);
    f32x4 d[8];
#pragma unroll
    for (int j = 0; j < 8; ++j) d[j] = mfma32(a0, Ff[j], zero4);
    u32 e[4];
#pragma unroll
    for (int j = 0; j < 4; ++j) {
      const float keep = c2 ? d[j + 4][1] : d[j][0], send = c2 ? d[j][1] : d[j + 4][0];
      e[j] = enc24(keep + dpp<XOR8>(send)) | tagl;
    }
    if (l < 16) {
      const int red = 64 * cg + 16 * (w & 3) + 2 * (l & 7) + c2;
      const int off = (int)(WS_IN + ((red * 128 + (w >> 2) * 64 + RG) * 4) * 8);
      const u32x4 g0 = {e[0], tagh, e[1], tagh}, g1 = {e[2], tagh, e[3], tagh};
      __builtin_amdgcn_raw_buffer_store_b128(g0, rs, off, 0, 16);
      __builtin_amdgcn_raw_buffer_store_b128(g1, rs, off + 16, 0, 16);
    }
  }
  STAMP(8);
  if (w == 0) {
    const int off = (int)(WS_IN + (b * 128 + l) * 32);
    u32x4 y[4];
    for (u32 spin = 0; spin < SPIN_CAP; ++spin) {
      asm volatile("" ::: "memory");
      y[0] = __builtin_amdgcn_raw_buffer_load_b128(rs, off, 0, 16);
      y[1] = __builtin_amdgcn_raw_buffer_load_b128(rs, off + 16, 0, 16);
      y[2] = __builtin_amdgcn_raw_buffer_load_b128(rs, off + 2048, 0, 16);
      y[3] = __builtin_amdgcn_raw_buffer_load_b128(rs, off + 2048 + 16, 0, 16);
      if (!__any((bad16(y[0]) | bad16(y[1])) | (bad16(y[2]) | bad16(y[3])))) break;
    }
    float ra = fold16(fold32(asf(y[0][0] << 8), asf(y[1][0] << 8)), fold32(asf(y[0][2] << 8), asf(y[1][2] << 8)));
    float rb = fold16(fold32(asf(y[2][0] << 8), asf(y[3][0] << 8)), fold32(asf(y[2][2] << 8), asf(y[3][2] << 8)));
    ra += dpp<XOR1>(ra); rb += dpp<XOR1>(rb); ra += dpp<XOR2>(ra); rb += dpp<XOR2>(rb);
    ra += dpp<XOR7>(ra); rb += dpp<XOR7>(rb); ra += dpp<XOR8>(ra); rb += dpp<XOR8>(rb);
    if ((l & 15) == 0) {
      const u32 xw = (g4 & 2) ? xfin[1] : xfin[0];
      const float xv = (g4 & 1) ? bfhi(xw) : bflo(xw);
      p.out[4 * b + g4] = (u16)f2bf(rbf(xv + rbf(ra)) + rbf(rb));
    }
    if (b == 0 && l == 0) __hip_atomic_store((gu64*)(p.ws + WS_EPOCH), E + 1, RLX_AGENT);
  }
  STAMP(9);
}

extern "C" void kernel_launch(void* const* d_in, const int* in_sizes, int n_in, void* d_out, int out_size, void* d_ws, size_t ws_size,
                              hipStream_t stream) {
  Params p;
  p.x = (const u16*)d_in[0];
  p.K = (const u16*)d_in[1];
  p.V = (const u16*)d_in[2];
  p.wq = (const u16*)d_in[3];
  p.wo = (const u16*)d_in[4];
  p.wg = (const u16*)d_in[6];
  p.wu = (const u16*)d_in[7];
  p.wd = (const u16*)d_in[8];
  p.out = (u16*)d_out;
  p.ws = (unsigned char*)d_ws;
  fused_decode<<<NWG, NT, 0, stream>>>(p);
}
```

```cpp
#include <hip/hip_runtime.h>
#include <hip/hip_bf16.h>
#include <stdint.h>

typedef unsigned u32;
typedef unsigned long long u64;
typedef unsigned short u16;
typedef u32 u32x4 __attribute__((ext_vector_type(4)));
typedef u32 u32x2 __attribute__((ext_vector_type(2)));
typedef float f32x4 __attribute__((ext_vector_type(4)));
typedef float f32x2 __attribute__((ext_vector_type(2)));
typedef short bf16x8 __attribute__((ext_vector_type(8)));
typedef short bf16x4 __attribute__((ext_vector_type(4)));
#define GAS __attribute__((address_space(1)))
#define LAS __attribute__((address_space(3)))
typedef GAS u64 gu64;
#define RLX_AGENT __ATOMIC_RELAXED, __HIP_MEMORY_SCOPE_AGENT

constexpr int DM = 1024, SK = 65536, DH = 128, FF = 3584;
constexpr int NWG = 256, NW = 8, NT = NW * 64;
constexpr u32 WS_EPOCH = 0;
constexpr u32 WS_QP = 16384;
constexpr u32 WS_Q = WS_QP + 64 * 4 * 64 * 8;
constexpr u32 WS_GU = WS_Q + 4 * 1024 * 8;
constexpr u32 WS_LSE = WS_GU + 2 * 4 * FF * 8;
constexpr u32 WS_O = WS_LSE + 4 * 8 * 64 * 16;
constexpr u32 WS_IN = WS_O + 4 * 8 * 16 * 64 * 8 * 8;
constexpr u32 WS_END = WS_IN + 256 * 128 * 4 * 8;
constexpr u32 WS_DBG = WS_END;
constexpr u32 SPIN_CAP = 1u << 17;
constexpr u32 OOB = 0x40000000u;
#define POLL_SLEEP 1
constexpr int L_XN = 0;
constexpr int L_QS = L_XN + 2048;
constexpr int L_PT = L_QS + 2048;
constexpr int L_SM = L_PT + 4096;
constexpr int L_SL = L_SM + 256;
constexpr int L_OB = L_SL + 256;
constexpr int L_HB = L_OB + 128;
constexpr int L_FLAG = L_HB + 128;
constexpr int L_SO = L_FLAG + 128;
constexpr int L_BYTES = 88 * 1024;

struct Params {
  const u16* x; const u16* K; const u16* V; const u16* wq; const u16* wo; const u16* wg; const u16* wu; const u16* wd;
  u16* out; unsigned char* ws;
};

__device__ __forceinline__ float asf(u32 u) { return __builtin_bit_cast(float, u); }
__device__ __forceinline__ u32 asu(float f) { return __builtin_bit_cast(u32, f); }
__device__ __forceinline__ float bflo(u32 u) { return asf(u << 16); }
__device__ __forceinline__ float bfhi(u32 u) { return asf(u & 0xffff0000u); }
__device__ __forceinline__ float rbf(float f) { return (float)(__bf16)f; }
__device__ __forceinline__ u32 f2bf(float f) { return (u32)__builtin_bit_cast(u16, (__bf16)f); }
__device__ __forceinline__ u32 pkbf(float lo, float hi) {
  typedef __bf16 bf2 __attribute__((ext_vector_type(2)));
  f32x2 v = {lo, hi};
  return __builtin_bit_cast(u32, __builtin_convertvector(v, bf2));
}
__device__ __forceinline__ f32x4 mfma32(u32x4 a, u32x4 b, f32x4 c) {
  return __builtin_amdgcn_mfma_f32_16x16x32_bf16(__builtin_bit_cast(bf16x8, a), __builtin_bit_cast(bf16x8, b), c, 0, 0, 0);
}
__device__ __forceinline__ f32x4 mfma16(u32x2 a, u32x2 b, f32x4 c) {
  return __builtin_amdgcn_mfma_f32_16x16x16bf16_1k(__builtin_bit_cast(bf16x4, a), __builtin_bit_cast(bf16x4, b), c, 0, 0, 0);
}
template <int CTRL> __device__ __forceinline__ float dpp(float x) {
  return asf((u32)__builtin_amdgcn_mov_dpp(__builtin_bit_cast(int, x), CTRL, 0xf, 0xf, true));
}
constexpr int XOR1 = 0xB1, XOR2 = 0x4E, XOR7 = 0x141, XOR8 = 0x128, ROR4 = 0x124;
__device__ __forceinline__ float fold32(float a, float b) {
  auto r = __builtin_amdgcn_permlane32_swap(asu(a), asu(b), false, false);
  return asf((u32)r[0]) + asf((u32)r[1]);
}
__device__ __forceinline__ float fold16(float a, float b) {
  auto r = __builtin_amdgcn_permlane16_swap(asu(a), asu(b), false, false);
  return asf((u32)r[0]) + asf((u32)r[1]);
}
__device__ __forceinline__ float max32(float a) {
  auto r = __builtin_amdgcn_permlane32_swap(asu(a), asu(a), false, false);
  return fmaxf(asf((u32)r[0]), asf((u32)r[1]));
}
__device__ __forceinline__ float max16(float a) {
  auto r = __builtin_amdgcn_permlane16_swap(asu(a), asu(a), false, false);
  return fmaxf(asf((u32)r[0]), asf((u32)r[1]));
}
__device__ __forceinline__ float wave_sum(float v) {
  v += dpp<XOR1>(v); v += dpp<XOR2>(v); v += dpp<XOR7>(v); v += dpp<XOR8>(v);
  v = fold16(v, v); v = fold32(v, v);
  return v;
}
__device__ __forceinline__ float wave_max(float v) {
  v = fmaxf(v, dpp<XOR1>(v)); v = fmaxf(v, dpp<XOR2>(v)); v = fmaxf(v, dpp<XOR7>(v)); v = fmaxf(v, dpp<XOR8>(v));
  return max32(max16(v));
}
__device__ __forceinline__ u32x4 ldnt(const unsigned char* p) { return __builtin_nontemporal_load((const u32x4*)p); }
#define SBAR() __builtin_amdgcn_sched_barrier(0)
__device__ __forceinline__ void wg_barrier() {
  asm volatile("s_waitcnt lgkmcnt(0)" ::: "memory");
  __builtin_amdgcn_s_barrier();
  asm volatile("" ::: "memory");
}
__device__ __forceinline__ u32 enc24(float v) { return (asu(v) + 0x80u) >> 8; }

__device__ __forceinline__ void transpose8(const u32x4 (&T)[8], u32x4 (&Bf)[8]) {
#pragma unroll
  for (int j = 0; j < 8; ++j) {
    const u32 sel = (j & 1) ? 0x07060302u : 0x05040100u;
#pragma unroll
    for (int u = 0; u < 4; ++u) Bf[j][u] = __builtin_amdgcn_perm(T[2 * u + 1][j >> 1], T[2 * u][j >> 1], sel);
  }
}

#define STAMP(i) do { } while (0)

struct Unit { u32x4 k[4]; u32x4 v[4]; };

__global__ void __launch_bounds__(NT, 2) fused_decode(Params p) {
  __shared__ __attribute__((aligned(16))) unsigned char smem[L_BYTES];
  const int tid = threadIdx.x, l = tid & 63, w = __builtin_amdgcn_readfirstlane(tid >> 6);
  const int b = blockIdx.x, g = b >> 6, s = b & 63;
  const __amdgpu_buffer_rsrc_t rs = __builtin_amdgcn_make_buffer_rsrc((void*)p.ws, 0, (int)WS_END, 0x00020000);
  const __amdgpu_buffer_rsrc_t rk = __builtin_amdgcn_make_buffer_rsrc((void*)(p.K + (size_t)g * SK * DH), 0, SK * DH * 2, 0x00020000);
  const __amdgpu_buffer_rsrc_t rv = __builtin_amdgcn_make_buffer_rsrc((void*)(p.V + (size_t)g * SK * DH), 0, SK * DH * 2, 0x00020000);
  STAMP(0);
  const int g4 = l >> 4, c2 = (l >> 3) & 1;
  const int koff = ((l & 15) >> 1) * 256 + (2 * g4 + (l & 1)) * 16 + (s * 8 + w) * 4096;
  const int voff = g4 * 512 + (l & 15) * 16 + (s * 8 + w) * 4096;
  auto issueK = [&](Unit& U, int k) {
    SBAR();
    const int so = k * (512 * 4096);
    U.k[0] = __builtin_amdgcn_raw_buffer_load_b128(rk, koff, so, 2); U.k[1] = __builtin_amdgcn_raw_buffer_load_b128(rk, koff + 128, so, 2);
    U.k[2] = __builtin_amdgcn_raw_buffer_load_b128(rk, koff + 2048, so, 2); U.k[3] = __builtin_amdgcn_raw_buffer_load_b128(rk, koff + 2048 + 128, so, 2);
    SBAR();
  };
  auto issueV = [&](Unit& U, int k) {
    SBAR();
    const int so = k * (512 * 4096);
    U.v[0] = __builtin_amdgcn_raw_buffer_load_b128(rv, voff, so, 2); U.v[1] = __builtin_amdgcn_raw_buffer_load_b128(rv, voff + 256, so, 2);
    U.v[2] = __builtin_amdgcn_raw_buffer_load_b128(rv, voff + 2048, so, 2); U.v[3] = __builtin_amdgcn_raw_buffer_load_b128(rv, voff + 2048 + 256, so, 2);
    SBAR();
  };
  Unit A, B, C, D;
  const f32x4 zero4 = {0.f, 0.f, 0.f, 0.f};
  volatile LAS u32* qflag = (volatile LAS u32*)(smem + L_FLAG);
  auto qready = [&]() -> bool { return __builtin_amdgcn_readfirstlane(*qflag) != 0u; };

  const int cgq = b >> 2, kq = b & 3, R0 = 256 * kq + 32 * w;
  const u32x4 xa = *(const u32x4*)(p.x + 16 * l), xb = *(const u32x4*)(p.x + 16 * l + 8);
  const u32 xr = *(const u32*)(p.x + 128 * w + 2 * l);
  const u32x2 xq = *(const u32x2*)(p.x + R0 + 4 * (2 * g4 + (l & 1)));
  SBAR();
  u32x4 T[4];
  {
    const unsigned char* wb = (const unsigned char*)(p.wq + (size_t)R0 * 4096 + cgq * 64) + (u32)(4 * (l >> 3)) * 8192u + (u32)(l & 7) * 16u;
#pragma unroll
    for (int i = 0; i < 4; ++i) T[i] = ldnt(wb + (size_t)i * 8192);
  }
  const u64 E = __hip_atomic_load((gu64*)(p.ws + WS_EPOCH), RLX_AGENT);
  issueK(A, 0);
  if (tid == 0) *qflag = 0u;
  {
    float xs[16];
#pragma unroll
    for (int j = 0; j < 4; ++j) { xs[2 * j] = bflo(xa[j]); xs[2 * j + 1] = bfhi(xa[j]); xs[8 + 2 * j] = bflo(xb[j]); xs[9 + 2 * j] = bfhi(xb[j]); }
    float sm = 0.f;
#pragma unroll
    for (int j = 0; j < 16; ++j) sm += xs[j];
    const float mean = wave_sum(sm) * (1.f / DM);
    float sv = 0.f;
#pragma unroll
    for (int j = 0; j < 16; ++j) { const float d = xs[j] - mean; sv += d * d; }
    const float var = wave_sum(sv) * (1.f / DM);
    const float mb = rbf(mean), den = rbf(sqrtf(rbf(rbf(var) + 1e-5f)));
    u32x2 aq;
    aq[0] = pkbf(rbf(bflo(xq[0]) - mb) / den, rbf(bfhi(xq[0]) - mb) / den);
    aq[1] = pkbf(rbf(bflo(xq[1]) - mb) / den, rbf(bfhi(xq[1]) - mb) / den);
    f32x4 d[8];
#pragma unroll
    for (int j = 0; j < 8; ++j) {
      const u32 sel = (j & 1) ? 0x07060302u : 0x05040100u;
      u32x2 f;
      f[0] = __builtin_amdgcn_perm(T[1][j >> 1], T[0][j >> 1], sel);
      f[1] = __builtin_amdgcn_perm(T[3][j >> 1], T[2][j >> 1], sel);
      d[j] = mfma16(aq, f, zero4);
    }
    issueV(A, 0);
    if (l < 16) {
      f32x4 lo, hi;
#pragma unroll
      for (int j = 0; j < 4; ++j) { lo[j] = c2 ? d[j][1] : d[j][0]; hi[j] = c2 ? d[j + 4][1] : d[j + 4][0]; }
      *(f32x4*)(smem + L_PT + ((w * 2 + c2) * 64 + 8 * (l & 7)) * 4) = lo;
      *(f32x4*)(smem + L_PT + ((w * 2 + c2) * 64 + 8 * (l & 7) + 4) * 4) = hi;
    }
    *(u32*)(smem + L_XN + (64 * w + l) * 4) = pkbf(rbf(bflo(xr) - mb) / den, rbf(bfhi(xr) - mb) / den);
  }
  const u32 tagl = (u32)(E + 1) << 24, tagh = (u32)((E + 1) >> 8);
  auto bad16 = [&](const u32x4& x) -> u32 { return (x[1] ^ tagh) | (x[3] ^ tagh) | (((x[0] ^ tagl) | (x[2] ^ tagl)) >> 24); };
  wg_barrier();
  STAMP(1);
  if (w == 0) {
    const float* pt = (const float*)(smem + L_PT);
    float a[16];
#pragma unroll
    for (int i = 0; i < 16; ++i) a[i] = pt[i * 64 + l];
#pragma unroll
    for (int st = 8; st > 0; st >>= 1)
#pragma unroll
      for (int i = 0; i < st; ++i) a[i] += a[i + st];
    const u32x2 gr = {enc24(a[0]) | tagl, tagh};
    __builtin_amdgcn_raw_buffer_store_b64(gr, rs, (int)(WS_QP + ((cgq * 4 + kq) * 64 + l) * 8), 0, 16);
    const int off = (int)(WS_QP + ((cgq * 4 + g4) * 64 + 16 * kq + (l & 15)) * 8);
    u32x2 x;
    for (u32 spin = 0; spin < SPIN_CAP; ++spin) {
      asm volatile("" ::: "memory");
      x = __builtin_amdgcn_raw_buffer_load_b64(rs, off, 0, 16);
      if (!__any((x[1] ^ tagh) | ((x[0] ^ tagl) >> 24))) break;
      __builtin_amdgcn_s_sleep(2);
    }
    float v = asf(x[0] << 8);
    v = fold16(v, v); v = fold32(v, v);
    const u32x2 gq = {(asu(rbf(v)) >> 8) | tagl, tagh};
    if (l < 16) __builtin_amdgcn_raw_buffer_store_b64(gq, rs, (int)(WS_Q + (cgq * 64 + 16 * kq + l) * 8), 0, 16);
  }
  STAMP(2);
#define PACE() do { if (!qready()) asm volatile("s_waitcnt vmcnt(4)" ::: "memory"); } while (0)
  PACE(); issueK(B, 1);
  PACE(); issueV(B, 1);
  if (w == 7) {
    const int off = (int)(WS_Q + (g * 1024 + 2 * l) * 8);
    u32x4 q[8];
#pragma unroll
    for (int i = 0; i < 8; ++i) q[i] = __builtin_amdgcn_raw_buffer_load_b128(rs, off + i * 1024, 0, 16);
    for (u32 spin = 0; spin < SPIN_CAP; ++spin) {
      asm volatile("" ::: "memory");
      u32 bd[8], any = 0;
#pragma unroll
      for (int i = 0; i < 8; ++i) { bd[i] = bad16(q[i]); any |= bd[i]; }
      if (!__any(any)) break;
      __builtin_amdgcn_s_sleep(2);
#pragma unroll
      for (int i = 0; i < 8; ++i)
        if (bd[i]) q[i] = __builtin_amdgcn_raw_buffer_load_b128(rs, off + i * 1024, 0, 16);
    }
#pragma unroll
    for (int i = 0; i < 8; ++i) *(u32*)(smem + L_QS + (i * 64 + l) * 4) = ((q[i][0] >> 8) & 0xffffu) | ((q[i][2] << 8) & 0xffff0000u);
    asm volatile("s_waitcnt lgkmcnt(0)" ::: "memory");
    *qflag = 1u;
  }
  PACE(); issueK(C, 2);
  PACE(); issueV(C, 2);
  PACE(); issueK(D, 3);
  PACE(); issueV(D, 3);
  for (u32 spin = 0; spin < (SPIN_CAP << 4) && !qready(); ++spin) __builtin_amdgcn_s_sleep(1);
  asm volatile("" ::: "memory");
  STAMP(3);
  u32x4 qf[2];
#pragma unroll
  for (int s2 = 0; s2 < 2; ++s2) qf[s2] = *(const u32x4*)(smem + L_QS + ((l & 7) * 128 + (8 * s2 + 2 * g4 + c2) * 8) * 2);

  float m = -3.0e38f, lsum = 0.f;
  f32x4 acc[8];
#pragma unroll
  for (int j = 0; j < 8; ++j) acc[j] = zero4;
  u32x2 pa;
  auto qk = [&](const Unit& U) {
    f32x4 d0 = mfma32(U.k[0], qf[0], zero4); d0 = mfma32(U.k[1], qf[1], d0);
    f32x4 d1 = mfma32(U.k[2], qf[0], zero4); d1 = mfma32(U.k[3], qf[1], d1);
    float t0 = (c2 ? d0[3] : d0[0]) + dpp<XOR8>(c2 ? d0[1] : d0[2]);
    float t1 = (c2 ? d1[3] : d1[0]) + dpp<XOR8>(c2 ? d1[1] : d1[2]);
    t0 = rbf(rbf(t0) * 0.08838834764831845f) * 1.4426950408889634f;
    t1 = rbf(rbf(t1) * 0.08838834764831845f) * 1.4426950408889634f;
    const float mx = fmaxf(t0, t1);
    if (__any(mx > m)) {
      float mn = fmaxf(mx, dpp<XOR8>(mx));
      mn = fmaxf(max32(max16(mn)), m);
      const float al = __builtin_amdgcn_exp2f(m - mn);
      m = mn; lsum *= al;
      float ar[4];
#pragma unroll
      for (int r = 0; r < 4; ++r) {
        const float lo = asf((u32)__builtin_amdgcn_readlane((int)asu(al), r)), hi = asf((u32)__builtin_amdgcn_readlane((int)asu(al), 4 + r));
        ar[r] = (g4 & 1) ? hi : lo;
      }
#pragma unroll
      for (int j = 0; j < 8; ++j)
#pragma unroll
        for (int r = 0; r < 4; ++r) acc[j][r] *= ar[r];
    }
    const float p0 = __builtin_amdgcn_exp2f(t0 - m), p1 = __builtin_amdgcn_exp2f(t1 - m);
    lsum += p0 + p1;
    const u32 pk = pkbf(p0, p1);
    pa[0] = c2 ? 0u : pk; pa[1] = c2 ? pk : 0u;
  };
  auto pv = [&](const Unit& U) {
#pragma unroll
    for (int j = 0; j < 8; ++j) {
      const u32 sel = (j & 1) ? 0x07060302u : 0x05040100u;
      u32x2 f;
      f[0] = __builtin_amdgcn_perm(U.v[2][j >> 1], U.v[0][j >> 1], sel);
      f[1] = __builtin_amdgcn_perm(U.v[3][j >> 1], U.v[1][j >> 1], sel);
      acc[j] = mfma16(pa, f, acc[j]);
    }
  };
  const bool hasgu = s >= 8;
  const int tgu = g * 56 + (hasgu ? s - 8 : 0), mat = tgu / 112, tt = tgu % 112, cg2 = tt >> 2, ks = tt & 3;
  const int rgl = s >> 2, cg = s & 3, RG = g * 16 + rgl;
  const u32 arow = (u32)(8 * (2 * g4 + (l & 1))) * 2u;
  u32x4 G[8], F[8];
#define LOCKSTEP() __builtin_amdgcn_s_barrier()
  qk(A); issueK(A, 4); pv(A); issueV(A, 4);
  qk(B); issueK(B, 5); pv(B); issueV(B, 5);
  LOCKSTEP();
  qk(C); issueK(C, 6); pv(C); issueV(C, 6);
  qk(D); issueK(D, 7); pv(D); issueV(D, 7);
  LOCKSTEP();
  qk(A); pv(A);
  {
    SBAR();
    const __amdgpu_buffer_rsrc_t rg = __builtin_amdgcn_make_buffer_rsrc((void*)(mat ? p.wu : p.wg), 0, DM * FF * 2, 0x00020000);
    const int o = hasgu ? (int)((u32)(256 * ks + 64 * (w >> 1) + 8 * (l >> 3)) * 7168u + (u32)(cg2 * 128 + (w & 1) * 64 + (l & 7) * 8) * 2u) : (int)OOB;
#pragma unroll
    for (int i = 0; i < 8; ++i) G[i] = __builtin_amdgcn_raw_buffer_load_b128(rg, o, i * 7168, 2);
    SBAR();
  }
  qk(B); pv(B);
  LOCKSTEP();
  {
    SBAR();
    const bool dn = w >= 4;
    const __amdgpu_buffer_rsrc_t rf = __builtin_amdgcn_make_buffer_rsrc((void*)(dn ? p.wd : p.wo), 0, dn ? FF * DM * 2 : 4096 * DM * 2, 0x00020000);
    u32 o = (u32)((dn ? 56 : 64) * RG + 8 * (l >> 3)) * 2048u + (u32)(256 * cg + 64 * (w & 3) + (l & 7) * 8) * 2u;
    if (dn && l >= 56) o = OOB;
#pragma unroll
    for (int i = 0; i < 8; ++i) F[i] = __builtin_amdgcn_raw_buffer_load_b128(rf, (int)o, i * 2048, 2);
    SBAR();
  }
  qk(C); pv(C);
  qk(D); pv(D);
  STAMP(4);

  {
    float ls = lsum + dpp<XOR8>(lsum);
    ls = fold16(ls, ls); ls = fold32(ls, ls);
    if (l < 8) { *(float*)(smem + L_SM + (l * 8 + w) * 4) = m; *(float*)(smem + L_SL + (l * 8 + w) * 4) = ls; }
#pragma unroll
    for (int r = 0; r < 4; ++r) {
      f32x4 o;
#pragma unroll
      for (int j = 0; j < 4; ++j) o[j] = fold32(acc[j][r], acc[j + 4][r]);
      *(f32x4*)(smem + L_SO + ((w * 8 + 4 * (g4 & 1) + r) * 128 + 8 * (l & 15) + 4 * (l >> 5)) * 4) = o;
    }
  }
  wg_barrier();
  {
    const f32x4 ma = *(const f32x4*)(smem + L_SM + w * 32), mb = *(const f32x4*)(smem + L_SM + w * 32 + 16);
    const f32x4 la = *(const f32x4*)(smem + L_SL + w * 32), lb = *(const f32x4*)(smem + L_SL + w * 32 + 16);
    const float M = fmaxf(fmaxf(fmaxf(ma[0], ma[1]), fmaxf(ma[2], ma[3])), fmaxf(fmaxf(mb[0], mb[1]), fmaxf(mb[2], mb[3])));
    float L = 0.f, o0 = 0.f, o1 = 0.f;
#pragma unroll
    for (int i = 0; i < 8; ++i) {
      const float wt = __builtin_amdgcn_exp2f((i < 4 ? ma[i & 3] : mb[i & 3]) - M);
      const f32x2 ov = *(const f32x2*)(smem + L_SO + ((i * 8 + w) * 128 + 2 * l) * 4);
      L = fmaf(wt, i < 4 ? la[i & 3] : lb[i & 3], L); o0 = fmaf(wt, ov[0], o0); o1 = fmaf(wt, ov[1], o1);
    }
    const float inv = 1.f / L;
    const u32x4 g0 = {enc24(o0 * inv) | tagl, tagh, enc24(o1 * inv) | tagl, tagh};
    __builtin_amdgcn_raw_buffer_store_b128(g0, rs, (int)(WS_O + ((((g * 8 + w) * 16 + (l >> 2)) * 64 + s) * 8 + 2 * (l & 3)) * 8), 0, 16);
    if (l == 0) {
      const float lse = M + __builtin_amdgcn_logf(L);
      const u32 hi = enc24(lse);
      const u32x4 gl = {hi | tagl, tagh, enc24(lse - asf(hi << 8)) | tagl, tagh};
      __builtin_amdgcn_raw_buffer_store_b128(gl, rs, (int)(WS_LSE + ((g * 8 + w) * 64 + s) * 16), 0, 16);
    }
  }
  SBAR();
  STAMP(5);
  if (hasgu) {
    const u32x4 a0 = *(const u32x4*)(smem + L_XN + (256 * ks + 64 * (w >> 1)) * 2 + arow);
    u32x4 Bf[8];
    transpose8(G, Bf);
    f32x4 d[8];
#pragma unroll
    for (int j = 0; j < 8; ++j) d[j] = mfma32(a0, Bf[j], zero4);
    if (l < 16) {
      f32x4 lo, hi;
#pragma unroll
      for (int j = 0; j < 4; ++j) { lo[j] = c2 ? d[j][1] : d[j][0]; hi[j] = c2 ? d[j + 4][1] : d[j + 4][0]; }
      *(f32x4*)(smem + L_PT + (((w >> 1) * 2 + c2) * 128 + (w & 1) * 64 + 8 * (l & 7)) * 4) = lo;
      *(f32x4*)(smem + L_PT + (((w >> 1) * 2 + c2) * 128 + (w & 1) * 64 + 8 * (l & 7) + 4) * 4) = hi;
    }
    wg_barrier();
    if (w < 2) {
      const float* pt = (const float*)(smem + L_PT) + w * 64 + l;
      const float r = ((pt[0] + pt[128]) + (pt[256] + pt[384])) + ((pt[512] + pt[640]) + (pt[768] + pt[896]));
      const u32x2 gr = {enc24(r) | tagl, tagh};
      __builtin_amdgcn_raw_buffer_store_b64(gr, rs, (int)(WS_GU + ((mat * 4 + ks) * FF + cg2 * 128 + w * 64 + l) * 8), 0, 16);
    }
  }
  STAMP(6);
  const u32x2 xfin = *(const u32x2*)(p.x + 4 * b);
  u32x4 Ff[8];
  transpose8(F, Ff);
#pragma unroll
  for (int j = 0; j < 8; ++j) asm volatile("" : "+v"(Ff[j]));
  STAMP(7);
  {
    const int hd = rgl >> 1, half = rgl & 1;
    const int offl = (int)(WS_LSE + ((g * 8 + hd) * 64 + (l >> 2)) * 16);
    const int offo = (int)(WS_O + (((g * 8 + hd) * 16 + half * 8 + w) * 64 * 8 + 2 * l) * 8);
    u32x4 xl[4], xo[4];
#pragma unroll
    for (int i = 0; i < 4; ++i) {
      xl[i] = __builtin_amdgcn_raw_buffer_load_b128(rs, offl + i * 256, 0, 16);
      xo[i] = __builtin_amdgcn_raw_buffer_load_b128(rs, offo + i * 1024, 0, 16);
    }
    for (u32 spin = 0; spin < SPIN_CAP; ++spin) {
      asm volatile("" ::: "memory");
      u32 bd[4], any = 0;
#pragma unroll
      for (int i = 0; i < 4; ++i) { bd[i] = bad16(xl[i]) | bad16(xo[i]); any |= bd[i]; }
      if (!__any(any)) break;
      __builtin_amdgcn_s_sleep(POLL_SLEEP);
#pragma unroll
      for (int i = 0; i < 4; ++i)
        if (bd[i]) {
          xl[i] = __builtin_amdgcn_raw_buffer_load_b128(rs, offl + i * 256, 0, 16);
          xo[i] = __builtin_amdgcn_raw_buffer_load_b128(rs, offo + i * 1024, 0, 16);
        }
    }
    float lse[4];
#pragma unroll
    for (int i = 0; i < 4; ++i) lse[i] = asf(xl[i][0] << 8) + asf(xl[i][2] << 8);
    float M = fmaxf(fmaxf(lse[0], lse[1]), fmaxf(lse[2], lse[3]));
    M = fmaxf(M, dpp<ROR4>(M)); M = fmaxf(M, dpp<XOR8>(M)); M = max32(max16(M));
    float W = 0.f, o0 = 0.f, o1 = 0.f;
#pragma unroll
    for (int i = 0; i < 4; ++i) {
      const float wt = __builtin_amdgcn_exp2f(lse[i] - M);
      W += wt; o0 = fmaf(wt, asf(xo[i][0] << 8), o0); o1 = fmaf(wt, asf(xo[i][2] << 8), o1);
    }
    float o = fold32(o0, o1);
    W = fold32(W, W);
    o = fold16(o, o); W = fold16(W, W);
    o += dpp<XOR8>(o); W += dpp<XOR8>(W);
    o += dpp<ROR4>(o); W += dpp<ROR4>(W);
    if ((l & 28) == 0) *(u16*)(smem + L_OB + (8 * w + 2 * (l & 3) + (l >> 5)) * 2) = (u16)f2bf(o / W);
  }
  if (w == 7) {
    const int fidx = 56 * RG + (l < 56 ? l : 0);
    u32x2 gx[8];
    for (u32 spin = 0; spin < SPIN_CAP; ++spin) {
      asm volatile("" ::: "memory");
      u32 bad = 0;
#pragma unroll
      for (int i = 0; i < 8; ++i) gx[i] = __builtin_amdgcn_raw_buffer_load_b64(rs, (int)(WS_GU + (i * FF + fidx) * 8), 0, 16);
#pragma unroll
      for (int i = 0; i < 8; ++i) bad |= (gx[i][1] ^ tagh) | ((gx[i][0] ^ tagl) >> 24);
      if (!__any(bad)) break;
      __builtin_amdgcn_s_sleep(POLL_SLEEP);
    }
    float a[8];
#pragma unroll
    for (int i = 0; i < 8; ++i) a[i] = asf(gx[i][0] << 8);
    const float gt = rbf((a[0] + a[1]) + (a[2] + a[3])), up = rbf((a[4] + a[5]) + (a[6] + a[7]));
    const float ge = rbf(0.5f * gt * (1.f + tanhf(0.7978845608028654f * (gt + 0.044715f * gt * gt * gt))));
    *(u16*)(smem + L_HB + l * 2) = l < 56 ? (u16)f2bf(ge * up) : (u16)0;
  }
  wg_barrier();
  STAMP(8);
  {
    const u32x4 a0 = *(const u32x4*)(smem + (w < 4 ? L_OB : L_HB) + arow);
    f32x4 d[8];
#pragma unroll
    for (int j = 0; j < 8; ++j) d[j] = mfma32(a0, Ff[j], zero4);
    u32 e[4];
#pragma unroll
    for (int j = 0; j < 4; ++j) {
      const float keep = c2 ? d[j + 4][1] : d[j][0], send = c2 ? d[j][1] : d[j + 4][0];
      e[j] = enc24(keep + dpp<XOR8>(send)) | tagl;
    }
    if (l < 16) {
      const int red = 64 * cg + 16 * (w & 3) + 2 * (l & 7) + c2;
      const int off = (int)(WS_IN + ((red * 128 + (w >> 2) * 64 + RG) * 4) * 8);
      const u32x4 g0 = {e[0], tagh, e[1], tagh}, g1 = {e[2], tagh, e[3], tagh};
      __builtin_amdgcn_raw_buffer_store_b128(g0, rs, off, 0, 16);
      __builtin_amdgcn_raw_buffer_store_b128(g1, rs, off + 16, 0, 16);
    }
  }
  STAMP(9);
  if (w == 0) {
    const int off = (int)(WS_IN + (b * 128 + l) * 32);
    u32x4 y[4];
    y[0] = __builtin_amdgcn_raw_buffer_load_b128(rs, off, 0, 16);
    y[1] = __builtin_amdgcn_raw_buffer_load_b128(rs, off + 16, 0, 16);
    y[2] = __builtin_amdgcn_raw_buffer_load_b128(rs, off + 2048, 0, 16);
    y[3] = __builtin_amdgcn_raw_buffer_load_b128(rs, off + 2048 + 16, 0, 16);
    for (u32 spin = 0; spin < SPIN_CAP; ++spin) {
      asm volatile("" ::: "memory");
      const u32 b0 = bad16(y[0]) | bad16(y[1]), b1 = bad16(y[2]) | bad16(y[3]);
      if (!__any(b0 | b1)) break;
      __builtin_amdgcn_s_sleep(POLL_SLEEP);
      if (b0) {
        y[0] = __builtin_amdgcn_raw_buffer_load_b128(rs, off, 0, 16);
        y[1] = __builtin_amdgcn_raw_buffer_load_b128(rs, off + 16, 0, 16);
      }
      if (b1) {
        y[2] = __builtin_amdgcn_raw_buffer_load_b128(rs, off + 2048, 0, 16);
        y[3] = __builtin_amdgcn_raw_buffer_load_b128(rs, off + 2048 + 16, 0, 16);
      }
    }
    float ra = fold16(fold32(asf(y[0][0] << 8), asf(y[1][0] << 8)), fold32(asf(y[0][2] << 8), asf(y[1][2] << 8)));
    float rb = fold16(fold32(asf(y[2][0] << 8), asf(y[3][0] << 8)), fold32(asf(y[2][2] << 8), asf(y[3][2] << 8)));
    ra += dpp<XOR1>(ra); rb += dpp<XOR1>(rb); ra += dpp<XOR2>(ra); rb += dpp<XOR2>(rb);
    ra += dpp<XOR7>(ra); rb += dpp<XOR7>(rb); ra += dpp<XOR8>(ra); rb += dpp<XOR8>(rb);
    if ((l & 15) == 0) {
      const u32 xw = (g4 & 2) ? xfin[1] : xfin[0];
      const float xv = (g4 & 1) ? bfhi(xw) : bflo(xw);
      p.out[4 * b + g4] = (u16)f2bf(rbf(xv + rbf(ra)) + rbf(rb));
    }
    if (b == 0 && l == 0) __hip_atomic_store((gu64*)(p.ws + WS_EPOCH), E + 1, RLX_AGENT);
  }
  STAMP(10);
}

extern "C" void kernel_launch(void* const* d_in, const int* in_sizes, int n_in, void* d_out, int out_size, void* d_ws, size_t ws_size,
                              hipStream_t stream) {
  Params p;
  p.x = (const u16*)d_in[0];
  p.K = (const u16*)d_in[1];
  p.V = (const u16*)d_in[2];
  p.wq = (const u16*)d_in[3];
  p.wo = (const u16*)d_in[4];
  p.wg = (const u16*)d_in[6];
  p.wu = (const u16*)d_in[7];
  p.wd = (const u16*)d_in[8];
  p.out = (u16*)d_out;
  p.ws = (unsigned char*)d_ws;
  fused_decode<<<NWG, NT, 0, stream>>>(p);
}
```

```cpp
#include <hip/hip_runtime.h>
#include <hip/hip_bf16.h>
#include <stdint.h>

typedef unsigned u32;
typedef unsigned long long u64;
typedef unsigned short u16;
typedef u32 u32x4 __attribute__((ext_vector_type(4)));
typedef u32 u32x2 __attribute__((ext_vector_type(2)));
typedef float f32x4 __attribute__((ext_vector_type(4)));
typedef float f32x2 __attribute__((ext_vector_type(2)));
typedef short bf16x8 __attribute__((ext_vector_type(8)));
typedef short bf16x4 __attribute__((ext_vector_type(4)));
#define GAS __attribute__((address_space(1)))
#define LAS __attribute__((address_space(3)))
typedef GAS u64 gu64;
#define RLX_AGENT __ATOMIC_RELAXED, __HIP_MEMORY_SCOPE_AGENT

constexpr int DM = 1024, SK = 65536, DH = 128, FF = 3584;
constexpr int NWG = 256, NW = 8, NT = NW * 64;
constexpr u32 WS_EPOCH = 0;
constexpr u32 WS_QP = 4096;
constexpr u32 WS_GU = WS_QP + 4 * 4 * 1024 * 4;
constexpr u32 WS_LSE = WS_GU + 2 * 8 * FF * 4;
constexpr u32 WS_O = WS_LSE + 4 * 8 * 64 * 16;
constexpr u32 WS_OUT = WS_O + 4 * 8 * 2 * 64 * 64 * 4;
constexpr u32 WS_END = WS_OUT + 64 * 1024 * 4;
constexpr u32 WS_DBG = WS_END;
constexpr u32 SPIN_CAP = 1u << 17;
constexpr u32 OOB = 0x40000000u;
#define PW 8
#define PS 4
#define GSTART 13
constexpr int L_XN = 0;
constexpr int L_QS = L_XN + 2048;
constexpr int L_PT = L_QS + 2048;
constexpr int L_SM = L_PT + 2048;
constexpr int L_SL = L_SM + 128;
constexpr int L_OP = L_SL + 128;
constexpr int L_OB = L_OP + 1024;
constexpr int L_HB = L_OB + 512;
constexpr int L_FP = L_HB + 128;
constexpr int L_CNT = L_FP + 256;
constexpr int L_SO = L_CNT + 128;
constexpr int L_BYTES = 88 * 1024;
enum { C_QP = 0, C_Q = 1, C_G = 2, C_MRG = 3, C_O = 4, C_H = 5, C_FIN = 6, C_TAG = 8 };

struct Params {
  const u16* x; const u16* K; const u16* V; const u16* wq; const u16* wo; const u16* wg; const u16* wu; const u16* wd;
  u16* out; unsigned char* ws;
};

__device__ __forceinline__ float asf(u32 u) { return __builtin_bit_cast(float, u); }
__device__ __forceinline__ u32 asu(float f) { return __builtin_bit_cast(u32, f); }
__device__ __forceinline__ float bflo(u32 u) { return asf(u << 16); }
__device__ __forceinline__ float bfhi(u32 u) { return asf(u & 0xffff0000u); }
__device__ __forceinline__ float rbf(float f) { return (float)(__bf16)f; }
__device__ __forceinline__ u32 f2bf(float f) { return (u32)__builtin_bit_cast(u16, (__bf16)f); }
__device__ __forceinline__ u32 pkbf(float lo, float hi) {
  typedef __bf16 bf2 __attribute__((ext_vector_type(2)));
  f32x2 v = {lo, hi};
  return __builtin_bit_cast(u32, __builtin_convertvector(v, bf2));
}
__device__ __forceinline__ f32x4 mfma32(u32x4 a, u32x4 b, f32x4 c) {
  return __builtin_amdgcn_mfma_f32_16x16x32_bf16(__builtin_bit_cast(bf16x8, a), __builtin_bit_cast(bf16x8, b), c, 0, 0, 0);
}
__device__ __forceinline__ f32x4 mfma16(u32x2 a, u32x2 b, f32x4 c) {
  return __builtin_amdgcn_mfma_f32_16x16x16bf16_1k(__builtin_bit_cast(bf16x4, a), __builtin_bit_cast(bf16x4, b), c, 0, 0, 0);
}
template <int CTRL> __device__ __forceinline__ float dpp(float x) {
  return asf((u32)__builtin_amdgcn_mov_dpp(__builtin_bit_cast(int, x), CTRL, 0xf, 0xf, true));
}
constexpr int XOR1 = 0xB1, XOR2 = 0x4E, XOR7 = 0x141, XOR8 = 0x128, ROR4 = 0x124;
__device__ __forceinline__ float fold32(float a, float b) {
  auto r = __builtin_amdgcn_permlane32_swap(asu(a), asu(b), false, false);
  return asf((u32)r[0]) + asf((u32)r[1]);
}
__device__ __forceinline__ float fold16(float a, float b) {
  auto r = __builtin_amdgcn_permlane16_swap(asu(a), asu(b), false, false);
  return asf((u32)r[0]) + asf((u32)r[1]);
}
__device__ __forceinline__ float max32(float a) {
  auto r = __builtin_amdgcn_permlane32_swap(asu(a), asu(a), false, false);
  return fmaxf(asf((u32)r[0]), asf((u32)r[1]));
}
__device__ __forceinline__ float max16(float a) {
  auto r = __builtin_amdgcn_permlane16_swap(asu(a), asu(a), false, false);
  return fmaxf(asf((u32)r[0]), asf((u32)r[1]));
}
__device__ __forceinline__ float wave_sum(float v) {
  v += dpp<XOR1>(v); v += dpp<XOR2>(v); v += dpp<XOR7>(v); v += dpp<XOR8>(v);
  v = fold16(v, v); v = fold32(v, v);
  return v;
}
__device__ __forceinline__ float wave_max(float v) {
  v = fmaxf(v, dpp<XOR1>(v)); v = fmaxf(v, dpp<XOR2>(v)); v = fmaxf(v, dpp<XOR7>(v)); v = fmaxf(v, dpp<XOR8>(v));
  return max32(max16(v));
}
#define SBAR() __builtin_amdgcn_sched_barrier(0)
#define STR2(x) #x
#define STR(x) STR2(x)
#define VMWAIT(n) asm volatile("s_waitcnt vmcnt(" STR(n) ")" ::: "memory")
__device__ __forceinline__ u32 enc24(float v) { return (asu(v) + 0x80u) >> 8; }

__device__ __forceinline__ void transpose8(const u32x4 (&T)[8], u32x4 (&Bf)[8]) {
#pragma unroll
  for (int j = 0; j < 8; ++j) {
    const u32 sel = (j & 1) ? 0x07060302u : 0x05040100u;
#pragma unroll
    for (int u = 0; u < 4; ++u) Bf[j][u] = __builtin_amdgcn_perm(T[2 * u + 1][j >> 1], T[2 * u][j >> 1], sel);
  }
}

#define STAMP(i) do { } while (0)

struct Unit { u32x4 k[4]; u32x4 v[4]; };

__global__ void __launch_bounds__(NT, 2) fused_decode(Params p) {
  __shared__ __attribute__((aligned(16))) unsigned char smem[L_BYTES];
  const int tid = threadIdx.x, l = tid & 63, w = __builtin_amdgcn_readfirstlane(tid >> 6), j = w & 3;
  const int b = blockIdx.x, g = b >> 6, s = b & 63;
  const __amdgpu_buffer_rsrc_t rs = __builtin_amdgcn_make_buffer_rsrc((void*)p.ws, 0, (int)WS_END, 0x00020000);
  STAMP(0);
  const int g4 = l >> 4, c2 = (l >> 3) & 1;
  const f32x4 zero4 = {0.f, 0.f, 0.f, 0.f};
  volatile LAS u32* cnt = (volatile LAS u32*)(smem + L_CNT);
  auto sig = [&](int c) {
    asm volatile("s_waitcnt lgkmcnt(0)" ::: "memory");
    if (l == 0) __hip_atomic_fetch_add((LAS u32*)(smem + L_CNT) + c, 1u, __ATOMIC_RELAXED, __HIP_MEMORY_SCOPE_WORKGROUP);
  };
  auto await = [&](int c, u32 n) {
    for (u32 spin = 0; spin < (1u << 24); ++spin) {
      if ((u32)__builtin_amdgcn_readfirstlane(cnt[c]) >= n) break;
      __builtin_amdgcn_s_sleep(1);
    }
    asm volatile("" ::: "memory");
  };
  const int rgl = s >> 2, cg = s & 3, RG = g * 16 + rgl;

  if (w < 4) {
    const __amdgpu_buffer_rsrc_t rk = __builtin_amdgcn_make_buffer_rsrc((void*)(p.K + (size_t)g * SK * DH), 0, SK * DH * 2, 0x00020000);
    const __amdgpu_buffer_rsrc_t rv = __builtin_amdgcn_make_buffer_rsrc((void*)(p.V + (size_t)g * SK * DH), 0, SK * DH * 2, 0x00020000);
    const int koff = ((l & 15) >> 1) * 256 + (2 * g4 + (l & 1)) * 16 + (s * 4 + j) * 4096;
    const int voff = g4 * 512 + (l & 15) * 16 + (s * 4 + j) * 4096;
    auto issueK = [&](Unit& U, int k) {
      SBAR();
      const int so = k << 20;
      U.k[0] = __builtin_amdgcn_raw_buffer_load_b128(rk, koff, so, 2); U.k[1] = __builtin_amdgcn_raw_buffer_load_b128(rk, koff + 128, so, 2);
      U.k[2] = __builtin_amdgcn_raw_buffer_load_b128(rk, koff + 2048, so, 2); U.k[3] = __builtin_amdgcn_raw_buffer_load_b128(rk, koff + 2048 + 128, so, 2);
      SBAR();
    };
    auto issueV = [&](Unit& U, int k) {
      SBAR();
      const int so = k << 20;
      U.v[0] = __builtin_amdgcn_raw_buffer_load_b128(rv, voff, so, 2); U.v[1] = __builtin_amdgcn_raw_buffer_load_b128(rv, voff + 256, so, 2);
      U.v[2] = __builtin_amdgcn_raw_buffer_load_b128(rv, voff + 2048, so, 2); U.v[3] = __builtin_amdgcn_raw_buffer_load_b128(rv, voff + 2048 + 256, so, 2);
      SBAR();
    };
    Unit A, B, C, D;
    issueK(A, 0);
    __builtin_amdgcn_s_barrier();
    VMWAIT(PS); issueV(A, 0);
    VMWAIT(PS); issueK(B, 1);
    VMWAIT(PS); issueV(B, 1);
    VMWAIT(PS); issueK(C, 2);
    VMWAIT(PS); issueV(C, 2);
    VMWAIT(PS); issueK(D, 3);
    VMWAIT(PS); issueV(D, 3);
    STAMP(1);
    await(C_Q, 4);
    STAMP(2);
    u32x4 qf[2];
#pragma unroll
    for (int s2 = 0; s2 < 2; ++s2) qf[s2] = *(const u32x4*)(smem + L_QS + ((l & 7) * 128 + (8 * s2 + 2 * g4 + c2) * 8) * 2);
    const u32 t0 = cnt[C_TAG], t1 = cnt[C_TAG + 1], tagl = cnt[C_TAG + 2], tagh = cnt[C_TAG + 3];

    float m = -3.0e38f, lsum = 0.f;
    f32x4 acc[8];
#pragma unroll
    for (int i = 0; i < 8; ++i) acc[i] = zero4;
    auto process = [&](const Unit& U) {
      f32x4 d0 = mfma32(U.k[0], qf[0], zero4); d0 = mfma32(U.k[1], qf[1], d0);
      f32x4 d1 = mfma32(U.k[2], qf[0], zero4); d1 = mfma32(U.k[3], qf[1], d1);
      float s0 = (c2 ? d0[3] : d0[0]) + dpp<XOR8>(c2 ? d0[1] : d0[2]);
      float s1 = (c2 ? d1[3] : d1[0]) + dpp<XOR8>(c2 ? d1[1] : d1[2]);
      s0 = rbf(rbf(s0) * 0.08838834764831845f) * 1.4426950408889634f;
      s1 = rbf(rbf(s1) * 0.08838834764831845f) * 1.4426950408889634f;
      const float mx = fmaxf(s0, s1);
      if (__any(mx > m)) {
        float mn = fmaxf(mx, dpp<XOR8>(mx));
        mn = fmaxf(max32(max16(mn)), m);
        const float al = __builtin_amdgcn_exp2f(m - mn);
        m = mn; lsum *= al;
        float ar[4];
#pragma unroll
        for (int r = 0; r < 4; ++r) {
          const float lo = asf((u32)__builtin_amdgcn_readlane((int)asu(al), r)), hi = asf((u32)__builtin_amdgcn_readlane((int)asu(al), 4 + r));
          ar[r] = (g4 & 1) ? hi : lo;
        }
#pragma unroll
        for (int i = 0; i < 8; ++i)
#pragma unroll
          for (int r = 0; r < 4; ++r) acc[i][r] *= ar[r];
      }
      const float p0 = __builtin_amdgcn_exp2f(s0 - m), p1 = __builtin_amdgcn_exp2f(s1 - m);
      lsum += p0 + p1;
      const u32 pk = pkbf(p0, p1);
      u32x2 pa;
      pa[0] = c2 ? 0u : pk; pa[1] = c2 ? pk : 0u;
#pragma unroll
      for (int i = 0; i < 8; ++i) {
        const u32 sel = (i & 1) ? 0x07060302u : 0x05040100u;
        u32x2 f;
        f[0] = __builtin_amdgcn_perm(U.v[2][i >> 1], U.v[0][i >> 1], sel);
        f[1] = __builtin_amdgcn_perm(U.v[3][i >> 1], U.v[1][i >> 1], sel);
        acc[i] = mfma16(pa, f, acc[i]);
      }
    };
#define STEP(U, k) do { process(U); VMWAIT(PW); issueK(U, (k)); VMWAIT(PW); issueV(U, (k)); if ((k) == GSTART && j == 0) sig(C_G); } while (0)
#pragma unroll 1
    for (int k = 4; k < 16; k += 4) {
      STEP(A, k); STEP(B, k + 1); STEP(C, k + 2); STEP(D, k + 3);
    }
    process(A); process(B); process(C); process(D);
    STAMP(3);
    {
      float ls = lsum + dpp<XOR8>(lsum);
      ls = fold16(ls, ls); ls = fold32(ls, ls);
      if (l < 8) { *(float*)(smem + L_SM + (l * 4 + j) * 4) = m; *(float*)(smem + L_SL + (l * 4 + j) * 4) = ls; }
#pragma unroll
      for (int r = 0; r < 4; ++r) {
        f32x4 o;
#pragma unroll
        for (int i = 0; i < 4; ++i) o[i] = fold32(acc[i][r], acc[i + 4][r]);
        *(f32x4*)(smem + L_SO + ((j * 8 + 4 * (g4 & 1) + r) * 128 + 8 * (l & 15) + 4 * (l >> 5)) * 4) = o;
      }
    }
    sig(C_MRG);
    await(C_MRG, 4);
    {
      const int hd = 2 * j + (l >> 5), e = l & 31;
      const f32x4 mm = *(const f32x4*)(smem + L_SM + hd * 16), ll = *(const f32x4*)(smem + L_SL + hd * 16);
      const float M = fmaxf(fmaxf(mm[0], mm[1]), fmaxf(mm[2], mm[3]));
      float L = 0.f;
      f32x4 o = zero4;
#pragma unroll
      for (int i = 0; i < 4; ++i) {
        const float wt = __builtin_amdgcn_exp2f(mm[i] - M);
        L = fmaf(wt, ll[i], L);
        o += wt * *(const f32x4*)(smem + L_SO + ((i * 8 + hd) * 128 + 4 * e) * 4);
      }
      const float inv = 1.f / L;
      const u32x4 g0 = {enc24(o[0] * inv) | t0, enc24(o[1] * inv) | t1, enc24(o[2] * inv) | t0, enc24(o[3] * inv) | t1};
      __builtin_amdgcn_raw_buffer_store_b128(g0, rs, (int)(WS_O + ((((g * 8 + hd) * 2 + (e >> 4)) * 64 + s) * 64 + 4 * (e & 15)) * 4), 0, 16);
      if (e == 0) {
        const float lse = M + __builtin_amdgcn_logf(L);
        const u32 hi = enc24(lse);
        const u32x4 gl = {hi | tagl, tagh, enc24(lse - asf(hi << 8)) | tagl, tagh};
        __builtin_amdgcn_raw_buffer_store_b128(gl, rs, (int)(WS_LSE + ((g * 8 + hd) * 64 + s) * 16), 0, 16);
      }
    }
    STAMP(4);
    auto badd = [&](const u32x4& x) -> u32 { return (((x[0] ^ t0) | (x[1] ^ t1)) | ((x[2] ^ t0) | (x[3] ^ t1))) >> 24; };
    {
      const int hd = rgl >> 1, half = rgl & 1;
      const int offl = (int)(WS_LSE + ((g * 8 + hd) * 64 + l) * 16);
      const int offo = (int)(WS_O + ((((g * 8 + hd) * 2 + half) * 64 + 16 * j) * 64) * 4 + l * 16);
      u32x4 xl, xo[4];
      u32 pend = 0x1fu;
      for (u32 spin = 0; spin <= SPIN_CAP; ++spin) {
        asm volatile("" ::: "memory");
        if (pend & 16u) xl = __builtin_amdgcn_raw_buffer_load_b128(rs, offl, 0, 16);
#pragma unroll
        for (int i = 0; i < 4; ++i)
          if (pend & (1u << i)) xo[i] = __builtin_amdgcn_raw_buffer_load_b128(rs, offo + i * 1024, 0, 16);
        const u64 bl = __ballot(((xl[1] ^ tagh) | (xl[3] ^ tagh) | (((xl[0] ^ tagl) | (xl[2] ^ tagl)) >> 24)) != 0u);
        u32 np = bl ? 16u : 0u;
#pragma unroll
        for (int i = 0; i < 4; ++i) np |= __any(badd(xo[i])) ? (1u << i) : 0u;
        pend = np;
        if (!pend) break;
        if (__builtin_popcountll(bl) > 6) __builtin_amdgcn_s_sleep(16);
      }
      const float lse = asf(xl[0] << 8) + asf(xl[2] << 8);
      const float M = wave_max(lse);
      const float wt = __builtin_amdgcn_exp2f(lse - M);
      const float wn = wt / wave_sum(wt);
      f32x4 o = zero4;
#pragma unroll
      for (int i = 0; i < 4; ++i) {
        const float wp = __shfl(wn, 16 * j + 4 * i + g4);
#pragma unroll
        for (int u = 0; u < 4; ++u) o[u] = fmaf(wp, asf(xo[i][u] << 8), o[u]);
      }
      const float r = fold16(fold32(o[0], o[2]), fold32(o[1], o[3]));
      *(float*)(smem + L_OP + (j * 64 + 4 * (l & 15) + g4) * 4) = r;
    }
    sig(C_O);
    STAMP(5);
    if (b < 64) {
      const u32 xw = (l < 8) ? *(const u32*)(p.x + 16 * b + 2 * l) : 0u;
      const int off = (int)(WS_OUT + ((16 * j + (l >> 2)) * 1024 + 16 * b + 4 * (l & 3)) * 4);
      u32x4 y;
      for (u32 spin = 0; spin <= SPIN_CAP; ++spin) {
        asm volatile("" ::: "memory");
        y = __builtin_amdgcn_raw_buffer_load_b128(rs, off, 0, 16);
        const u64 bl = __ballot(badd(y) != 0u);
        if (!bl) break;
        if (__builtin_popcountll(bl) > 32) __builtin_amdgcn_s_sleep(8);
      }
      float r = fold16(fold32(asf(y[0] << 8), asf(y[2] << 8)), fold32(asf(y[1] << 8), asf(y[3] << 8)));
      r += dpp<XOR8>(r); r += dpp<ROR4>(r);
      if ((l & 12) == 0) *(float*)(smem + L_FP + (j * 16 + 4 * (l & 3) + g4) * 4) = r;
      sig(C_FIN);
      if (j == 0) {
        await(C_FIN, 4);
        if (l < 8) {
          const f32x2* fp = (const f32x2*)(smem + L_FP);
          const f32x2 a = (fp[l] + fp[8 + l]) + (fp[16 + l] + fp[24 + l]);
          *(u32*)(p.out + 16 * b + 2 * l) = pkbf(bflo(xw) + a[0], bfhi(xw) + a[1]);
        }
        if (b == 0 && l == 0) __hip_atomic_store((gu64*)(p.ws + WS_EPOCH), ((u64)cnt[C_TAG + 5] << 32) | cnt[C_TAG + 4], RLX_AGENT);
      }
    }
    STAMP(6);
  } else {
    const int cgq = b >> 2, kq = b & 3;
    const u32 arow = (u32)(8 * (2 * g4 + (l & 1))) * 2u;
    const u32x4 xa = *(const u32x4*)(p.x + 16 * l), xb = *(const u32x4*)(p.x + 16 * l + 8);
    const u32x2 xr = *(const u32x2*)(p.x + 256 * j + 4 * l);
    const u32x4 xq = *(const u32x4*)((const unsigned char*)(p.x + 256 * kq + 64 * j) + arow);
    SBAR();
    u32x4 T[8], Bf[8];
    {
      const __amdgpu_buffer_rsrc_t rq = __builtin_amdgcn_make_buffer_rsrc((void*)p.wq, 0, DM * 4096 * 2, 0x00020000);
      const int o = (256 * kq + 64 * j + 8 * (l >> 3)) * 8192 + (cgq * 64 + (l & 7) * 8) * 2;
#pragma unroll
      for (int i = 0; i < 8; ++i) T[i] = __builtin_amdgcn_raw_buffer_load_b128(rq, o, i * 8192, 2);
    }
    const u64 E = __hip_atomic_load((gu64*)(p.ws + WS_EPOCH), RLX_AGENT);
    SBAR();
    if (j == 0 && l < 8) cnt[l] = 0u;
    asm volatile("s_waitcnt lgkmcnt(0)" ::: "memory");
    __builtin_amdgcn_s_barrier();
    SBAR();
    f32x4 d[8];
    {
      float xs[16];
#pragma unroll
      for (int i = 0; i < 4; ++i) { xs[2 * i] = bflo(xa[i]); xs[2 * i + 1] = bfhi(xa[i]); xs[8 + 2 * i] = bflo(xb[i]); xs[9 + 2 * i] = bfhi(xb[i]); }
      float sm = 0.f;
#pragma unroll
      for (int i = 0; i < 16; ++i) sm += xs[i];
      const float mean = wave_sum(sm) * (1.f / DM);
      float sv = 0.f;
#pragma unroll
      for (int i = 0; i < 16; ++i) { const float dv = xs[i] - mean; sv += dv * dv; }
      const float var = wave_sum(sv) * (1.f / DM);
      const float mb = rbf(mean), den = rbf(sqrtf(rbf(rbf(var) + 1e-5f)));
      u32x4 aq;
#pragma unroll
      for (int i = 0; i < 4; ++i) aq[i] = pkbf(rbf(bflo(xq[i]) - mb) / den, rbf(bfhi(xq[i]) - mb) / den);
      transpose8(T, Bf);
#pragma unroll
      for (int i = 0; i < 8; ++i) d[i] = mfma32(aq, Bf[i], zero4);
      if (l < 16) {
        f32x4 lo, hi;
#pragma unroll
        for (int i = 0; i < 4; ++i) { lo[i] = c2 ? d[i][1] : d[i][0]; hi[i] = c2 ? d[i + 4][1] : d[i + 4][0]; }
        *(f32x4*)(smem + L_PT + ((j * 2 + c2) * 64 + 8 * (l & 7)) * 4) = lo;
        *(f32x4*)(smem + L_PT + ((j * 2 + c2) * 64 + 8 * (l & 7) + 4) * 4) = hi;
      }
      sig(C_QP);
      u32x2 xn;
      xn[0] = pkbf(rbf(bflo(xr[0]) - mb) / den, rbf(bfhi(xr[0]) - mb) / den);
      xn[1] = pkbf(rbf(bflo(xr[1]) - mb) / den, rbf(bfhi(xr[1]) - mb) / den);
      *(u32x2*)(smem + L_XN + (256 * j + 4 * l) * 2) = xn;
    }
    const u64 tg = E + 1;
    const u32 t0 = (u32)tg << 24, t1 = ((u32)tg << 16) & 0xff000000u;
    auto badd = [&](const u32x4& x) -> u32 { return (((x[0] ^ t0) | (x[1] ^ t1)) | ((x[2] ^ t0) | (x[3] ^ t1))) >> 24; };
    STAMP(1);
    if (j == 0) {
      if (l == 0) { cnt[C_TAG] = t0; cnt[C_TAG + 1] = t1; cnt[C_TAG + 2] = (u32)tg << 24; cnt[C_TAG + 3] = (u32)(tg >> 8); cnt[C_TAG + 4] = (u32)tg; cnt[C_TAG + 5] = (u32)(tg >> 32); }
      await(C_QP, 4);
      if (l < 16) {
        const f32x4* pt = (const f32x4*)(smem + L_PT) + l;
        const f32x4 a = ((pt[0] + pt[16]) + (pt[32] + pt[48])) + ((pt[64] + pt[80]) + (pt[96] + pt[112]));
        const u32x4 gq = {enc24(a[0]) | t0, enc24(a[1]) | t1, enc24(a[2]) | t0, enc24(a[3]) | t1};
        __builtin_amdgcn_raw_buffer_store_b128(gq, rs, (int)(WS_QP + (((cgq >> 4) * 4 + kq) * 1024 + (cgq & 15) * 64 + 4 * l) * 4), 0, 16);
      }
    }
    STAMP(2);
    {
      const int off = (int)(WS_QP + (g * 4 * 1024 + 256 * j + 4 * l) * 4);
      u32x4 q[4];
      u32 pend = 0xfu;
      for (u32 spin = 0; spin <= SPIN_CAP; ++spin) {
        asm volatile("" ::: "memory");
#pragma unroll
        for (int i = 0; i < 4; ++i)
          if (pend & (1u << i)) q[i] = __builtin_amdgcn_raw_buffer_load_b128(rs, off + i * 4096, 0, 16);
        u32 np = 0;
#pragma unroll
        for (int i = 0; i < 4; ++i) np |= __any(badd(q[i])) ? (1u << i) : 0u;
        pend = np;
        if (!pend) break;
        __builtin_amdgcn_s_sleep(4);
      }
      float v[4];
#pragma unroll
      for (int u = 0; u < 4; ++u) v[u] = (asf(q[0][u] << 8) + asf(q[1][u] << 8)) + (asf(q[2][u] << 8) + asf(q[3][u] << 8));
      const u32x2 qq = {pkbf(v[0], v[1]), pkbf(v[2], v[3])};
      *(u32x2*)(smem + L_QS + (256 * j + 4 * l) * 2) = qq;
    }
    sig(C_Q);
    STAMP(3);
    const bool hasgu = s >= 8;
    const int tgu = g * 56 + (hasgu ? s - 8 : 0), mat = tgu / 112, tt = tgu % 112, cg2 = tt >> 3, ks = tt & 7;
    const __amdgpu_buffer_rsrc_t rg = __builtin_amdgcn_make_buffer_rsrc((void*)(mat ? p.wu : p.wg), 0, DM * FF * 2, 0x00020000);
    const int go = hasgu ? (128 * ks + 8 * (l >> 3)) * 7168 + (cg2 * 256 + j * 64 + (l & 7) * 8) * 2 : (int)OOB;
    const __amdgpu_buffer_rsrc_t ro = __builtin_amdgcn_make_buffer_rsrc((void*)p.wo, 0, 4096 * DM * 2, 0x00020000);
    const __amdgpu_buffer_rsrc_t rd = __builtin_amdgcn_make_buffer_rsrc((void*)p.wd, 0, FF * DM * 2, 0x00020000);
    const int oo = (64 * RG + 8 * (l >> 3)) * 2048 + (256 * cg + 64 * j + (l & 7) * 8) * 2;
    const int dofs = l >= 56 ? (int)OOB : (56 * RG + 8 * (l >> 3)) * 2048 + (256 * cg + 64 * j + (l & 7) * 8) * 2;
    u32x4 G0[8], G1[8], FD[8], FO[8];
    await(C_G, 1);
    await(C_Q, 4);
    SBAR();
#pragma unroll
    for (int i = 0; i < 8; ++i) G0[i] = __builtin_amdgcn_raw_buffer_load_b128(rg, go, i * 7168, 2);
    SBAR(); VMWAIT(4); SBAR();
#pragma unroll
    for (int i = 0; i < 4; ++i) G1[i] = __builtin_amdgcn_raw_buffer_load_b128(rg, go, (64 + i) * 7168, 2);
    SBAR(); VMWAIT(4); SBAR();
#pragma unroll
    for (int i = 4; i < 8; ++i) G1[i] = __builtin_amdgcn_raw_buffer_load_b128(rg, go, (64 + i) * 7168, 2);
    SBAR();
    {
      const u32x4 a0 = *(const u32x4*)(smem + L_XN + (128 * ks) * 2 + arow), a1 = *(const u32x4*)(smem + L_XN + (128 * ks + 64) * 2 + arow);
      transpose8(G0, Bf);
#pragma unroll
      for (int i = 0; i < 8; ++i) d[i] = mfma32(a0, Bf[i], zero4);
      SBAR(); VMWAIT(4); SBAR();
#pragma unroll
      for (int i = 0; i < 4; ++i) FD[i] = __builtin_amdgcn_raw_buffer_load_b128(rd, dofs, i * 2048, 2);
      SBAR(); VMWAIT(4); SBAR();
#pragma unroll
      for (int i = 4; i < 8; ++i) FD[i] = __builtin_amdgcn_raw_buffer_load_b128(rd, dofs, i * 2048, 2);
      SBAR();
      transpose8(G1, Bf);
#pragma unroll
      for (int i = 0; i < 8; ++i) d[i] = mfma32(a1, Bf[i], d[i]);
      u32x4 e;
#pragma unroll
      for (int i = 0; i < 4; ++i) {
        const float keep = c2 ? d[i + 4][1] : d[i][0], send = c2 ? d[i][1] : d[i + 4][0];
        e[i] = enc24(keep + dpp<XOR8>(send)) | ((i & 1) ? t1 : t0);
      }
      if (l < 16 && hasgu)
        __builtin_amdgcn_raw_buffer_store_b128(e, rs, (int)(WS_GU + ((mat * 8 + ks) * FF + cg2 * 256 + j * 64 + 8 * (l & 7) + 4 * c2) * 4), 0, 16);
    }
    STAMP(4);
    SBAR(); VMWAIT(4); SBAR();
#pragma unroll
    for (int i = 0; i < 4; ++i) FO[i] = __builtin_amdgcn_raw_buffer_load_b128(ro, oo, i * 2048, 2);
    SBAR(); VMWAIT(4); SBAR();
#pragma unroll
    for (int i = 4; i < 8; ++i) FO[i] = __builtin_amdgcn_raw_buffer_load_b128(ro, oo, i * 2048, 2);
    SBAR();
    u32x4 Bd[8];
    transpose8(FD, Bd);
    transpose8(FO, Bf);
#pragma unroll
    for (int i = 0; i < 8; ++i) { asm volatile("" : "+v"(Bd[i])); asm volatile("" : "+v"(Bf[i])); }
    STAMP(5);
    if (j == 3) {
      const int e = (l & 15) < 14 ? (l & 15) : 0;
      const int off = (int)(WS_GU + (g4 * FF + 56 * RG + 4 * e) * 4);
      u32x4 x[4];
      for (u32 spin = 0; spin <= SPIN_CAP; ++spin) {
        asm volatile("" ::: "memory");
#pragma unroll
        for (int i = 0; i < 4; ++i) x[i] = __builtin_amdgcn_raw_buffer_load_b128(rs, off + i * (4 * FF * 4), 0, 16);
        if (!__any((badd(x[0]) | badd(x[1])) | (badd(x[2]) | badd(x[3])))) break;
        __builtin_amdgcn_s_sleep(2);
      }
      float gs[4], us[4];
#pragma unroll
      for (int u = 0; u < 4; ++u) { gs[u] = asf(x[0][u] << 8) + asf(x[1][u] << 8); us[u] = asf(x[2][u] << 8) + asf(x[3][u] << 8); }
      const float gt = rbf(fold16(fold32(gs[0], gs[2]), fold32(gs[1], gs[3])));
      const float up = rbf(fold16(fold32(us[0], us[2]), fold32(us[1], us[3])));
      const float ge = rbf(0.5f * gt * (1.f + tanhf(0.7978845608028654f * (gt + 0.044715f * gt * gt * gt))));
      *(u16*)(smem + L_HB + (4 * (l & 15) + g4) * 2) = (l & 15) < 14 ? (u16)f2bf(ge * up) : (u16)0;
      sig(C_H);
    }
    await(C_H, 1);
    {
      const u32x4 ah = *(const u32x4*)(smem + L_HB + arow);
#pragma unroll
      for (int i = 0; i < 8; ++i) d[i] = mfma32(ah, Bd[i], zero4);
    }
    STAMP(6);
    await(C_O, 4);
    STAMP(7);
    {
      const float* op = (const float*)(smem + L_OP) + l;
      *(u16*)(smem + L_OB + (j * 64 + l) * 2) = (u16)f2bf((op[0] + op[64]) + (op[128] + op[192]));
      asm volatile("s_waitcnt lgkmcnt(0)" ::: "memory");
      const u32x4 ao = *(const u32x4*)(smem + L_OB + j * 128 + arow);
#pragma unroll
      for (int i = 0; i < 8; ++i) d[i] = mfma32(ao, Bf[i], d[i]);
      u32x4 e;
#pragma unroll
      for (int i = 0; i < 4; ++i) {
        const float keep = c2 ? d[i + 4][1] : d[i][0], send = c2 ? d[i][1] : d[i + 4][0];
        e[i] = enc24(keep + dpp<XOR8>(send)) | ((i & 1) ? t1 : t0);
      }
      if (l < 16)
        __builtin_amdgcn_raw_buffer_store_b128(e, rs, (int)(WS_OUT + (RG * 1024 + 256 * cg + 64 * j + 8 * (l & 7) + 4 * c2) * 4), 0, 16);
    }
    STAMP(8);
  }
}

extern "C" void kernel_launch(void* const* d_in, const int* in_sizes, int n_in, void* d_out, int out_size, void* d_ws, size_t ws_size,
                              hipStream_t stream) {
  Params p;
  p.x = (const u16*)d_in[0];
  p.K = (const u16*)d_in[1];
  p.V = (const u16*)d_in[2];
  p.wq = (const u16*)d_in[3];
  p.wo = (const u16*)d_in[4];
  p.wg = (const u16*)d_in[6];
  p.wu = (const u16*)d_in[7];
  p.wd = (const u16*)d_in[8];
  p.out = (u16*)d_out;
  p.ws = (unsigned char*)d_ws;
  fused_decode<<<NWG, NT, 0, stream>>>(p);
}
```

```cpp
#include <hip/hip_runtime.h>
#include <hip/hip_bf16.h>
#include <stdint.h>

typedef unsigned u32;
typedef unsigned long long u64;
typedef unsigned short u16;
typedef u32 u32x4 __attribute__((ext_vector_type(4)));
typedef u32 u32x2 __attribute__((ext_vector_type(2)));
typedef float f32x4 __attribute__((ext_vector_type(4)));
typedef float f32x2 __attribute__((ext_vector_type(2)));
typedef short bf16x8 __attribute__((ext_vector_type(8)));
typedef short bf16x4 __attribute__((ext_vector_type(4)));
#define GAS __attribute__((address_space(1)))
#define LAS __attribute__((address_space(3)))
typedef GAS u64 gu64;
#define RLX_AGENT __ATOMIC_RELAXED, __HIP_MEMORY_SCOPE_AGENT

constexpr int DM = 1024, SK = 65536, DH = 128, FF = 3584;
constexpr int NWG = 256, NW = 8, NT = NW * 64;
constexpr u32 WS_EPOCH = 0;
constexpr u32 WS_CNT = 4096;
constexpr u32 WS_QP = 8192;
constexpr u32 WS_GU = WS_QP + 4 * 4 * 1024 * 4;
constexpr u32 WS_LSE = WS_GU + 2 * 16 * FF * 4;
constexpr u32 WS_O = WS_LSE + 4 * 8 * 64 * 16;
constexpr u32 WS_OUT = WS_O + 4 * 8 * 4 * 64 * 32 * 4;
constexpr u32 WS_END = WS_OUT + 128 * 1024 * 4;
constexpr u32 WS_DBG = WS_END;
constexpr u32 SPIN_CAP = 1u << 17;
constexpr u32 OOB = 0x40000000u;
#define PW 8
#define PS 2
#define KVD 0
#define KVDELAY()
#define GN 2
#define PG 8
#define H1SLEEP 16
#define Q0SLEEP 40
#define QSLEEP 4
#define H1SPEC 3
#define TLBW 1
#define PUBGATE 1
#define FSTART 15
#define GP 4
#define SKEW 0
#define TDELTA 120
#define TSLACK 25
#define FIGATE 0
#define GSTART 14
constexpr int L_XN = 0;
constexpr int L_QS = L_XN + 2048;
constexpr int L_PT = L_QS + 2048;
constexpr int L_SM = L_PT + 2048;
constexpr int L_SL = L_SM + 128;
constexpr int L_OP = L_SL + 128;
constexpr int L_OB = L_OP + 1024;
constexpr int L_HB = L_OB + 512;
constexpr int L_FP = L_HB + 128;
constexpr int L_MW = L_FP + 512;
constexpr int L_CNT = L_MW + 32;
constexpr int L_SO = L_CNT + 128;
constexpr int L_BYTES = 88 * 1024;
enum { C_QP = 0, C_Q = 1, C_G = 2, C_MRG = 3, C_O = 4, C_H = 5, C_FIN = 6, C_PUB = 7, C_F = 8, C_FI = 9, C_TAG = 16 };

struct Params {
  const u16* x; const u16* K; const u16* V; const u16* wq; const u16* wo; const u16* wg; const u16* wu; const u16* wd;
  u16* out; unsigned char* ws;
};

__device__ __forceinline__ float asf(u32 u) { return __builtin_bit_cast(float, u); }
__device__ __forceinline__ u32 asu(float f) { return __builtin_bit_cast(u32, f); }
__device__ __forceinline__ float bflo(u32 u) { return asf(u << 16); }
__device__ __forceinline__ float bfhi(u32 u) { return asf(u & 0xffff0000u); }
__device__ __forceinline__ float rbf(float f) { return (float)(__bf16)f; }
__device__ __forceinline__ u32 f2bf(float f) { return (u32)__builtin_bit_cast(u16, (__bf16)f); }
__device__ __forceinline__ u32 pkbf(float lo, float hi) {
  typedef __bf16 bf2 __attribute__((ext_vector_type(2)));
  f32x2 v = {lo, hi};
  return __builtin_bit_cast(u32, __builtin_convertvector(v, bf2));
}
__device__ __forceinline__ f32x4 mfma32(u32x4 a, u32x4 b, f32x4 c) {
  return __builtin_amdgcn_mfma_f32_16x16x32_bf16(__builtin_bit_cast(bf16x8, a), __builtin_bit_cast(bf16x8, b), c, 0, 0, 0);
}
__device__ __forceinline__ f32x4 mfma16(u32x2 a, u32x2 b, f32x4 c) {
  return __builtin_amdgcn_mfma_f32_16x16x16bf16_1k(__builtin_bit_cast(bf16x4, a), __builtin_bit_cast(bf16x4, b), c, 0, 0, 0);
}
template <int CTRL> __device__ __forceinline__ float dpp(float x) {
  return asf((u32)__builtin_amdgcn_mov_dpp(__builtin_bit_cast(int, x), CTRL, 0xf, 0xf, true));
}
constexpr int XOR1 = 0xB1, XOR2 = 0x4E, XOR7 = 0x141, XOR8 = 0x128, ROR4 = 0x124;
__device__ __forceinline__ float fold32(float a, float b) {
  auto r = __builtin_amdgcn_permlane32_swap(asu(a), asu(b), false, false);
  return asf((u32)r[0]) + asf((u32)r[1]);
}
__device__ __forceinline__ float fold16(float a, float b) {
  auto r = __builtin_amdgcn_permlane16_swap(asu(a), asu(b), false, false);
  return asf((u32)r[0]) + asf((u32)r[1]);
}
__device__ __forceinline__ float max32(float a) {
  auto r = __builtin_amdgcn_permlane32_swap(asu(a), asu(a), false, false);
  return fmaxf(asf((u32)r[0]), asf((u32)r[1]));
}
__device__ __forceinline__ float max16(float a) {
  auto r = __builtin_amdgcn_permlane16_swap(asu(a), asu(a), false, false);
  return fmaxf(asf((u32)r[0]), asf((u32)r[1]));
}
__device__ __forceinline__ float wave_sum(float v) {
  v += dpp<XOR1>(v); v += dpp<XOR2>(v); v += dpp<XOR7>(v); v += dpp<XOR8>(v);
  v = fold16(v, v); v = fold32(v, v);
  return v;
}
__device__ __forceinline__ float wave_max(float v) {
  v = fmaxf(v, dpp<XOR1>(v)); v = fmaxf(v, dpp<XOR2>(v)); v = fmaxf(v, dpp<XOR7>(v)); v = fmaxf(v, dpp<XOR8>(v));
  return max32(max16(v));
}
#define SBAR() __builtin_amdgcn_sched_barrier(0)
#define STR2(x) #x
#define STR(x) STR2(x)
#define VMWAIT(n) asm volatile("s_waitcnt vmcnt(" STR(n) ")" ::: "memory")
__device__ __forceinline__ float frcp(float v) { return __builtin_amdgcn_rcpf(v); }
__device__ __forceinline__ u32 enc24(float v) { return (asu(v) + 0x80u) >> 8; }

__device__ __forceinline__ void transpose8(const u32x4 (&T)[8], u32x4 (&Bf)[8]) {
#pragma unroll
  for (int j = 0; j < 8; ++j) {
    const u32 sel = (j & 1) ? 0x07060302u : 0x05040100u;
#pragma unroll
    for (int u = 0; u < 4; ++u) Bf[j][u] = __builtin_amdgcn_perm(T[2 * u + 1][j >> 1], T[2 * u][j >> 1], sel);
  }
}

__device__ __forceinline__ u32x4 pack_cols(const f32x4 (&d)[8], bool c2, u32 t0, u32 t1) {
  u32x4 e;
#pragma unroll
  for (int i = 0; i < 4; ++i) {
    const float x0 = d[i][0], x1 = d[i][1], y0 = d[i + 4][0], y1 = d[i + 4][1];
    const float keep = c2 ? y1 : x0, send = c2 ? x1 : y0;
    e[i] = enc24(keep + dpp<XOR8>(send)) | ((i & 1) ? t1 : t0);
  }
  return e;
}

__device__ __forceinline__ u32x4 pack_wide(const f32x4 (&d)[8], bool h, u32 t0, u32 t1) {
  u32x4 e;
#pragma unroll
  for (int i = 0; i < 4; ++i) {
    const float x0 = d[i][0], y0 = d[i + 4][0];
    e[i] = enc24(h ? y0 : x0) | ((i & 1) ? t1 : t0);
  }
  return e;
}

#define STAMP(i) do { } while (0)

struct Unit { u32x4 k[4]; u32x4 v[4]; };

__global__ void __launch_bounds__(NT, 2) fused_decode(Params p) {
  __shared__ __attribute__((aligned(16))) unsigned char smem[L_BYTES];
  const int tid = threadIdx.x, l = tid & 63, w = __builtin_amdgcn_readfirstlane(tid >> 6), j = w & 3;
  const int b = blockIdx.x, g = b >> 6, s = b & 63;
  const __amdgpu_buffer_rsrc_t rs = __builtin_amdgcn_make_buffer_rsrc((void*)p.ws, 0, (int)WS_END, 0x00020000);
  STAMP(0);
  const int g4 = l >> 4, c2 = (l >> 3) & 1;
  const f32x4 zero4 = {0.f, 0.f, 0.f, 0.f};
  volatile LAS u32* cnt = (volatile LAS u32*)(smem + L_CNT);
  auto sig = [&](int c) {
    asm volatile("s_waitcnt lgkmcnt(0)" ::: "memory");
    if (l == 0) __hip_atomic_fetch_add((LAS u32*)(smem + L_CNT) + c, 1u, __ATOMIC_RELAXED, __HIP_MEMORY_SCOPE_WORKGROUP);
  };
  auto await = [&](int c, u32 n) {
#pragma unroll 1
    for (u32 spin = 0; spin < (1u << 24); ++spin) {
      if ((u32)__builtin_amdgcn_readfirstlane(cnt[c]) >= n) break;
      __builtin_amdgcn_s_sleep(1);
    }
    asm volatile("" ::: "memory");
  };
  const int rq = s >> 1, cg = s & 1, RG = g * 32 + rq;

  if (w < 4) {
    const __amdgpu_buffer_rsrc_t rk = __builtin_amdgcn_make_buffer_rsrc((void*)(p.K + (size_t)g * SK * DH), 0, SK * DH * 2, 0x00020000);
    const __amdgpu_buffer_rsrc_t rv = __builtin_amdgcn_make_buffer_rsrc((void*)(p.V + (size_t)g * SK * DH), 0, SK * DH * 2, 0x00020000);
    const int koff = ((l & 15) >> 1) * 256 + (2 * g4 + (l & 1)) * 16 + (s * 4 + j) * 4096;
    const int voff = g4 * 512 + (l & 15) * 16 + (s * 4 + j) * 4096;
    auto issueK = [&](Unit& U, int k) {
      SBAR();
      const int so = k << 20;
      U.k[0] = __builtin_amdgcn_raw_buffer_load_b128(rk, koff, so, 2); U.k[1] = __builtin_amdgcn_raw_buffer_load_b128(rk, koff + 128, so, 2);
      U.k[2] = __builtin_amdgcn_raw_buffer_load_b128(rk, koff + 2048, so, 2); U.k[3] = __builtin_amdgcn_raw_buffer_load_b128(rk, koff + 2048 + 128, so, 2);
      SBAR();
    };
    auto issueV = [&](Unit& U, int k) {
      SBAR();
      const int so = k << 20;
      U.v[0] = __builtin_amdgcn_raw_buffer_load_b128(rv, voff, so, 2); U.v[1] = __builtin_amdgcn_raw_buffer_load_b128(rv, voff + 256, so, 2);
      U.v[2] = __builtin_amdgcn_raw_buffer_load_b128(rv, voff + 2048, so, 2); U.v[3] = __builtin_amdgcn_raw_buffer_load_b128(rv, voff + 2048 + 256, so, 2);
      SBAR();
    };
    const bool odd = SKEW && (s & 1);
    const int tl15 = SKEW ? (odd ? (384 + (s >> 1) * 4 + j) * 2048 : 3 * ((s >> 1) * 4 + j) * 2048) - (s * 4 + j) * 4096 : 0;
    auto issueKx = [&](Unit& U, int so, int adj, bool lo, bool hi) {
      SBAR();
      const int o0 = lo ? koff + adj : (int)OOB, o1 = hi ? koff + adj + 2048 : (int)OOB;
      U.k[0] = __builtin_amdgcn_raw_buffer_load_b128(rk, o0, so, 2); U.k[1] = __builtin_amdgcn_raw_buffer_load_b128(rk, o0 + 128, so, 2);
      U.k[2] = __builtin_amdgcn_raw_buffer_load_b128(rk, o1, so, 2); U.k[3] = __builtin_amdgcn_raw_buffer_load_b128(rk, o1 + 128, so, 2);
      SBAR();
    };
    auto issueVx = [&](Unit& U, int so, int adj, bool lo, bool hi) {
      SBAR();
      const int o0 = lo ? voff + adj : (int)OOB, o1 = hi ? voff + adj + 2048 : (int)OOB;
      U.v[0] = __builtin_amdgcn_raw_buffer_load_b128(rv, o0, so, 2); U.v[1] = __builtin_amdgcn_raw_buffer_load_b128(rv, o0 + 256, so, 2);
      U.v[2] = __builtin_amdgcn_raw_buffer_load_b128(rv, o1, so, 2); U.v[3] = __builtin_amdgcn_raw_buffer_load_b128(rv, o1 + 256, so, 2);
      SBAR();
    };
    Unit A, B, C, D;
    __builtin_amdgcn_s_barrier();
    issueK(A, 0);
    KVDELAY();
#define PACE0() do { if ((u32)__builtin_amdgcn_readfirstlane(cnt[C_Q]) < 4u) VMWAIT(PS); else VMWAIT(PW); } while (0)
    PACE0(); issueV(A, 0);
    PACE0(); issueK(B, 1);
    PACE0(); issueV(B, 1);
    PACE0(); issueK(C, 2);
    PACE0(); issueV(C, 2);
    PACE0(); issueK(D, 3);
    PACE0(); issueV(D, 3);
    STAMP(1);
    await(C_Q, 4);
    STAMP(2);
    u32x4 qf[2];
#pragma unroll
    for (int s2 = 0; s2 < 2; ++s2) qf[s2] = *(const u32x4*)(smem + L_QS + ((l & 7) * 128 + (8 * s2 + 2 * g4 + c2) * 8) * 2);
    const u32 t0 = cnt[C_TAG], t1 = cnt[C_TAG + 1], tagl = cnt[C_TAG + 2], tagh = cnt[C_TAG + 3];

    const u32 tq = (u32)__builtin_amdgcn_s_memrealtime();
#define TPACE(k) do { const u32 due = tq + (u32)(((k) - 4) * TDELTA - TSLACK); for (int z_ = 0; z_ < 256 && (int)((u32)__builtin_amdgcn_s_memrealtime() - due) < 0; ++z_) __builtin_amdgcn_s_sleep(1); } while (0)
    float m = -3.0e38f, lsum = 0.f;
    f32x4 acc[8];
#pragma unroll
    for (int i = 0; i < 8; ++i) acc[i] = zero4;
    auto process = [&](const Unit& U, bool lo = true, bool hi = true) {
      f32x4 d0 = mfma32(U.k[0], qf[0], zero4); d0 = mfma32(U.k[1], qf[1], d0);
      f32x4 d1 = mfma32(U.k[2], qf[0], zero4); d1 = mfma32(U.k[3], qf[1], d1);
      float s0 = (c2 ? d0[3] : d0[0]) + dpp<XOR8>(c2 ? d0[1] : d0[2]);
      float s1 = (c2 ? d1[3] : d1[0]) + dpp<XOR8>(c2 ? d1[1] : d1[2]);
      s0 = rbf(rbf(s0) * 0.08838834764831845f) * 1.4426950408889634f;
      s1 = rbf(rbf(s1) * 0.08838834764831845f) * 1.4426950408889634f;
      if (!lo) s0 = -3.0e38f;
      if (!hi) s1 = -3.0e38f;
      const float mx = fmaxf(s0, s1);
      if (__any(mx > m)) {
        float mn = fmaxf(mx, dpp<XOR8>(mx));
        mn = fmaxf(max32(max16(mn)), m);
        const float al = __builtin_amdgcn_exp2f(m - mn);
        m = mn; lsum *= al;
        float ar[4];
#pragma unroll
        for (int r = 0; r < 4; ++r) {
          const float lo = asf((u32)__builtin_amdgcn_readlane((int)asu(al), r)), hi = asf((u32)__builtin_amdgcn_readlane((int)asu(al), 4 + r));
          ar[r] = (g4 & 1) ? hi : lo;
        }
#pragma unroll
        for (int i = 0; i < 8; ++i)
#pragma unroll
          for (int r = 0; r < 4; ++r) acc[i][r] *= ar[r];
      }
      const float p0 = __builtin_amdgcn_exp2f(s0 - m), p1 = __builtin_amdgcn_exp2f(s1 - m);
      lsum += p0 + p1;
      const u32 pk = pkbf(p0, p1);
      u32x2 pa;
      pa[0] = c2 ? 0u : pk; pa[1] = c2 ? pk : 0u;
#pragma unroll
      for (int i = 0; i < 8; ++i) {
        const u32 sel = (i & 1) ? 0x07060302u : 0x05040100u;
        u32x2 f;
        f[0] = __builtin_amdgcn_perm(U.v[2][i >> 1], U.v[0][i >> 1], sel);
        f[1] = __builtin_amdgcn_perm(U.v[3][i >> 1], U.v[1][i >> 1], sel);
        acc[i] = mfma16(pa, f, acc[i]);
      }
    };
#define STEP(U, k) do { process(U); VMWAIT(PW); TPACE(k); issueK(U, (k)); VMWAIT(PW); issueV(U, (k)); if ((k) == GSTART) sig(C_G); if ((k) == FSTART) sig(C_F); } while (0)
#pragma unroll 1
    for (int k = 4; k < 16; k += 4) {
      STEP(A, k); STEP(B, k + 1); STEP(C, k + 2);
      process(D);
      const int adj = k == 12 ? tl15 : 0;
      const bool hi = !(k == 12 && odd);
      VMWAIT(PW); TPACE(k + 3); issueKx(D, (k + 3) << 20, adj, true, hi); VMWAIT(PW); issueVx(D, (k + 3) << 20, adj, true, hi);
      if (k + 3 == GSTART) sig(C_G);
      if (k + 3 == FSTART) sig(C_F);
    }
    process(A);
    if (SKEW) { VMWAIT(PW); issueKx(A, 15 << 20, tl15 + 4096, !odd, false); issueVx(A, 15 << 20, tl15 + 4096, !odd, false); }
    const int offl = (int)(WS_LSE + ((g * 8 + (rq >> 2)) * 64 + 16 * j + (l & 15)) * 16);
    const int offo = (int)(WS_O + (((g * 32 + rq) * 64 + 16 * j) * 32) * 4 + l * 16);
    u32x4 xl = __builtin_amdgcn_raw_buffer_load_b128(rs, offl, 0, 16);
    SBAR();
    process(B); process(C); process(D, true, !odd);
    if (SKEW) process(A, !odd, false);
    STAMP(3);
    {
      float ls = lsum + dpp<XOR8>(lsum);
      ls = fold16(ls, ls); ls = fold32(ls, ls);
      if (l < 8) { *(float*)(smem + L_SM + (l * 4 + j) * 4) = m; *(float*)(smem + L_SL + (l * 4 + j) * 4) = ls; }
#pragma unroll
      for (int r = 0; r < 4; ++r) {
        f32x4 o;
#pragma unroll
        for (int i = 0; i < 4; ++i) o[i] = fold32(acc[i][r], acc[i + 4][r]);
        *(f32x4*)(smem + L_SO + ((j * 8 + 4 * (g4 & 1) + r) * 128 + 8 * (l & 15) + 4 * (l >> 5)) * 4) = o;
      }
    }
    sig(C_MRG);
    await(C_MRG, 4);
    {
      const int hd = 2 * j + (l >> 5), e = l & 31;
      const f32x4 mm = *(const f32x4*)(smem + L_SM + hd * 16), ll = *(const f32x4*)(smem + L_SL + hd * 16);
      const float M = fmaxf(fmaxf(mm[0], mm[1]), fmaxf(mm[2], mm[3]));
      float L = 0.f;
      f32x4 o = zero4;
#pragma unroll
      for (int i = 0; i < 4; ++i) {
        const float wt = __builtin_amdgcn_exp2f(mm[i] - M);
        L = fmaf(wt, ll[i], L);
        o += wt * *(const f32x4*)(smem + L_SO + ((i * 8 + hd) * 128 + 4 * e) * 4);
      }
      const float inv = frcp(L);
      const u32x4 g0 = {enc24(o[0] * inv) | t0, enc24(o[1] * inv) | t1, enc24(o[2] * inv) | t0, enc24(o[3] * inv) | t1};
      __builtin_amdgcn_raw_buffer_store_b128(g0, rs, (int)(WS_O + ((((g * 8 + hd) * 4 + (e >> 3)) * 64 + s) * 32 + 4 * (e & 7)) * 4), 0, 16);
      if (e == 0) {
        const float lse = M + __builtin_amdgcn_logf(L);
        const u32 hi = enc24(lse);
        const u32x4 gl = {hi | tagl, tagh, enc24(lse - asf(hi << 8)) | tagl, tagh};
        __builtin_amdgcn_raw_buffer_store_b128(gl, rs, (int)(WS_LSE + ((g * 8 + hd) * 64 + s) * 16), 0, 16);
      }
    }
    sig(C_PUB);
    STAMP(4);
    auto badd = [&](const u32x4& x) -> u32 { return (((x[0] ^ t0) | (x[1] ^ t1)) | ((x[2] ^ t0) | (x[3] ^ t1))) >> 24; };
    {
      u32x4 xo[2];
      u32 need = 3u;
#pragma unroll 1
      for (u32 spin = 0; spin <= SPIN_CAP; ++spin) {
        asm volatile("" ::: "memory");
        const u32 lmiss = (u32)__ballot(((xl[1] ^ tagh) | (xl[3] ^ tagh) | (((xl[0] ^ tagl) | (xl[2] ^ tagl)) >> 24)) != 0u) & 0xffffu;
        if (!(need | lmiss)) break;
        const u32 gmiss = ((lmiss & 0xffu) ? 1u : 0u) | ((lmiss >> 8) ? 2u : 0u);
        const u32 fetch = (__builtin_popcount(lmiss) <= H1SPEC) ? need : (need & ~gmiss);
        if (!fetch) __builtin_amdgcn_s_sleep(H1SLEEP);
        if (lmiss) xl = __builtin_amdgcn_raw_buffer_load_b128(rs, offl, 0, 16);
#pragma unroll
        for (int i = 0; i < 2; ++i)
          if (fetch & (1u << i)) xo[i] = __builtin_amdgcn_raw_buffer_load_b128(rs, offo + i * 1024, 0, 16);
#pragma unroll
        for (int i = 0; i < 2; ++i)
          if ((fetch & (1u << i)) && !__any(badd(xo[i]))) need &= ~(1u << i);
      }
      const float lse = asf(xl[0] << 8) + asf(xl[2] << 8);
      float M = fmaxf(lse, dpp<XOR1>(lse)); M = fmaxf(M, dpp<XOR2>(M)); M = fmaxf(M, dpp<XOR7>(M)); M = fmaxf(M, dpp<XOR8>(M));
      const float wt = __builtin_amdgcn_exp2f(lse - M);
      float W = wt + dpp<XOR1>(wt); W += dpp<XOR2>(W); W += dpp<XOR7>(W); W += dpp<XOR8>(W);
      const float w0 = __shfl(wt, l >> 3), w1 = __shfl(wt, 8 + (l >> 3));
      f32x4 o;
#pragma unroll
      for (int u = 0; u < 4; ++u) o[u] = fmaf(w0, asf(xo[0][u] << 8), w1 * asf(xo[1][u] << 8));
      float r = fold16(fold32(o[0], o[2]), fold32(o[1], o[3]));
      r += dpp<XOR8>(r);
      if (!c2) *(float*)(smem + L_OP + (j * 32 + 4 * (l & 7) + g4) * 4) = r;
      if (l == 0) { *(float*)(smem + L_MW + j * 4) = M; *(float*)(smem + L_MW + 16 + j * 4) = W; }
    }
    sig(C_O);
    STAMP(5);
  } else {
    const int cgq = b >> 2, kq = b & 3;
    const u32 arow = (u32)(8 * (2 * g4 + (l & 1))) * 2u;
    const u32x4 xa = *(const u32x4*)(p.x + 16 * l), xb = *(const u32x4*)(p.x + 16 * l + 8);
    const u32x2 xr = *(const u32x2*)(p.x + 256 * j + 4 * l);
    const u32x4 xq = *(const u32x4*)((const unsigned char*)(p.x + 256 * kq + 64 * j) + arow);
    SBAR();
    u32x4 T[8], Bf[8];
    {
      const __amdgpu_buffer_rsrc_t rq = __builtin_amdgcn_make_buffer_rsrc((void*)p.wq, 0, DM * 4096 * 2, 0x00020000);
      const int o = (256 * kq + 64 * j + 8 * (l >> 3)) * 8192 + (cgq * 64 + (l & 7) * 8) * 2;
#pragma unroll
      for (int i = 0; i < 8; ++i) T[i] = __builtin_amdgcn_raw_buffer_load_b128(rq, o, i * 8192, 2);
    }
    const u64 E = __hip_atomic_load((gu64*)(p.ws + WS_EPOCH), RLX_AGENT);
    SBAR();
    if (j == 0 && l < 16) cnt[l] = 0u;
    asm volatile("s_waitcnt lgkmcnt(0)" ::: "memory");
    __builtin_amdgcn_s_barrier();
    SBAR();
    f32x4 d[8];
    {
      float xs[16];
#pragma unroll
      for (int i = 0; i < 4; ++i) { xs[2 * i] = bflo(xa[i]); xs[2 * i + 1] = bfhi(xa[i]); xs[8 + 2 * i] = bflo(xb[i]); xs[9 + 2 * i] = bfhi(xb[i]); }
      float sm = 0.f;
#pragma unroll
      for (int i = 0; i < 16; ++i) sm += xs[i];
      const float mean = wave_sum(sm) * (1.f / DM);
      float sv = 0.f;
#pragma unroll
      for (int i = 0; i < 16; ++i) { const float dv = xs[i] - mean; sv += dv * dv; }
      const float var = wave_sum(sv) * (1.f / DM);
      const float mb = rbf(mean), den = rbf(__builtin_amdgcn_sqrtf(rbf(rbf(var) + 1e-5f))), rden = frcp(den);
      auto nrm = [&](float xv) -> float { const float a = rbf(xv - mb), q0 = a * rden; return fmaf(fmaf(-q0, den, a), rden, q0); };
      u32x4 aq;
#pragma unroll
      for (int i = 0; i < 4; ++i) aq[i] = pkbf(nrm(bflo(xq[i])), nrm(bfhi(xq[i])));
      transpose8(T, Bf);
#pragma unroll
      for (int i = 0; i < 8; ++i) d[i] = mfma32(aq, Bf[i], zero4);
      if (l < 16) {
        f32x4 lo, hi;
#pragma unroll
        for (int i = 0; i < 4; ++i) { lo[i] = c2 ? d[i][1] : d[i][0]; hi[i] = c2 ? d[i + 4][1] : d[i + 4][0]; }
        *(f32x4*)(smem + L_PT + ((j * 2 + c2) * 64 + 8 * (l & 7)) * 4) = lo;
        *(f32x4*)(smem + L_PT + ((j * 2 + c2) * 64 + 8 * (l & 7) + 4) * 4) = hi;
      }
      sig(C_QP);
      u32x2 xn;
      xn[0] = pkbf(nrm(bflo(xr[0])), nrm(bfhi(xr[0])));
      xn[1] = pkbf(nrm(bflo(xr[1])), nrm(bfhi(xr[1])));
      *(u32x2*)(smem + L_XN + (256 * j + 4 * l) * 2) = xn;
    }
    const u64 tg = E + 1;
    const u32 t0 = (u32)tg << 24, t1 = ((u32)tg << 16) & 0xff000000u;
    auto badd = [&](const u32x4& x) -> u32 { return (((x[0] ^ t0) | (x[1] ^ t1)) | ((x[2] ^ t0) | (x[3] ^ t1))) >> 24; };
    STAMP(1);
    if (j == 0) {
      if (l == 0) { cnt[C_TAG] = t0; cnt[C_TAG + 1] = t1; cnt[C_TAG + 2] = (u32)tg << 24; cnt[C_TAG + 3] = (u32)(tg >> 8); cnt[C_TAG + 4] = (u32)tg; cnt[C_TAG + 5] = (u32)(tg >> 32); }
      await(C_QP, 4);
      if (l < 16) {
        const f32x4* pt = (const f32x4*)(smem + L_PT) + l;
        const f32x4 a = ((pt[0] + pt[16]) + (pt[32] + pt[48])) + ((pt[64] + pt[80]) + (pt[96] + pt[112]));
        const u32x4 gq = {enc24(a[0]) | t0, enc24(a[1]) | t1, enc24(a[2]) | t0, enc24(a[3]) | t1};
        __builtin_amdgcn_raw_buffer_store_b128(gq, rs, (int)(WS_QP + (((cgq >> 4) * 4 + kq) * 1024 + (cgq & 15) * 64 + 4 * l) * 4), 0, 16);
      }
    }
    STAMP(2);
    {
      const int off = (int)(WS_QP + (g * 4 * 1024 + 256 * j + 4 * l) * 4);
      u32x4 q[4];
      __builtin_amdgcn_s_sleep(Q0SLEEP);
#pragma unroll
      for (int i = 0; i < 4; ++i) q[i] = __builtin_amdgcn_raw_buffer_load_b128(rs, off + i * 4096, 0, 16);
#pragma unroll 1
      for (u32 spin = 0; spin <= SPIN_CAP; ++spin) {
        asm volatile("" ::: "memory");
        u64 bm[4];
#pragma unroll
        for (int i = 0; i < 4; ++i) bm[i] = __ballot(badd(q[i]) != 0u);
        if (!((bm[0] | bm[1]) | (bm[2] | bm[3]))) break;
        __builtin_amdgcn_s_sleep(QSLEEP);
#pragma unroll
        for (int i = 0; i < 4; ++i)
          if ((bm[i] >> (16 * g4)) & 0xffffull) q[i] = __builtin_amdgcn_raw_buffer_load_b128(rs, off + i * 4096, 0, 16);
      }
      float v[4];
#pragma unroll
      for (int u = 0; u < 4; ++u) v[u] = (asf(q[0][u] << 8) + asf(q[1][u] << 8)) + (asf(q[2][u] << 8) + asf(q[3][u] << 8));
      const u32x2 qq = {pkbf(v[0], v[1]), pkbf(v[2], v[3])};
      *(u32x2*)(smem + L_QS + (256 * j + 4 * l) * 2) = qq;
    }
    sig(C_Q);
    STAMP(3);
    const __amdgpu_buffer_rsrc_t rd = __builtin_amdgcn_make_buffer_rsrc((void*)p.wd, 0, FF * DM * 2, 0x00020000);
    const __amdgpu_buffer_rsrc_t rg = __builtin_amdgcn_make_buffer_rsrc((void*)(g >= 2 ? p.wu : p.wg), 0, DM * FF * 2, 0x00020000);
    const int oo = (32 * RG + 8 * g4) * 2048 + (512 * cg + 128 * j + (l & 15) * 8) * 2;
    const int dofs = (28 * RG + 7 * g4) * 2048 + (512 * cg + 128 * j + (l & 15) * 8) * 2;
    const int glane = 8 * g4 * 7168 + (l & 15) * 16;
    const __amdgpu_buffer_rsrc_t ro = __builtin_amdgcn_make_buffer_rsrc((void*)p.wo, 0, 4096 * DM * 2, 0x00020000);
    u32x4 G0[8], G1[8], FD[8];
    FD[7] = u32x4{0u, 0u, 0u, 0u};
    int hoff = (int)(WS_GU + ((l >> 3) * FF + 28 * RG + 4 * ((l & 7) < 7 ? (l & 7) : 0)) * 4);
    asm volatile("" : "+v"(hoff));
    const int xi = s & 7, xnone = 1 + 2 * ((s >> 3) & 3);
    const bool hasg = xi != xnone;
    const u32 ks = (u32)(g & 1) * 8u + (u32)(s >> 3), cs = (u32)(xi < xnone ? xi : xi - 1) * 4u + (u32)j;
    await(C_G, GN);
    await(C_Q, 4);
    {
      const int o = hasg ? glane + (int)(ks * (64u * 7168u) + cs * 256u) : (int)OOB;
      SBAR();
#pragma unroll
      for (int i = 0; i < 8; ++i) G0[i] = __builtin_amdgcn_raw_buffer_load_b128(rg, o, i * 7168, 2);
      SBAR(); VMWAIT(GP); SBAR();
#pragma unroll
      for (int i = 0; i < 4; ++i) G1[i] = __builtin_amdgcn_raw_buffer_load_b128(rg, o, (32 + i) * 7168, 2);
      SBAR(); VMWAIT(GP); SBAR();
#pragma unroll
      for (int i = 4; i < 8; ++i) G1[i] = __builtin_amdgcn_raw_buffer_load_b128(rg, o, (32 + i) * 7168, 2);
      SBAR();
      const u32x4 a0 = *(const u32x4*)(smem + L_XN + ks * 128u + g4 * 16), a1 = *(const u32x4*)(smem + L_XN + ks * 128u + 64u + g4 * 16);
      f32x4 dd[8];
      transpose8(G0, Bf);
#pragma unroll
      for (int i = 0; i < 8; ++i) dd[i] = mfma32(a0, Bf[i], zero4);
      transpose8(G1, Bf);
#pragma unroll
      for (int i = 0; i < 8; ++i) dd[i] = mfma32(a1, Bf[i], dd[i]);
      const u32x4 e = pack_wide(dd, g4 & 1, t0, t1);
      if (l < 32 && hasg)
        __builtin_amdgcn_raw_buffer_store_b128(e, rs, (int)(WS_GU + ((((u32)(g >> 1) * 16u + ks) * FF + cs * 128u + 8 * (l & 15) + 4 * g4) * 4)), 0, 16);
    }
    await(C_F, GN);
    SBAR();
#pragma unroll
    for (int i = 0; i < 7; ++i) FD[i] = __builtin_amdgcn_raw_buffer_load_b128(rd, dofs, i * 2048, 2);
    SBAR();
    await(C_PUB, 4);
    SBAR();
#pragma unroll
    for (int i = 0; i < 8; ++i) G0[i] = __builtin_amdgcn_raw_buffer_load_b128(ro, oo, i * 2048, 2);
    SBAR();
    sig(C_FI);
    STAMP(4);
    u32x4 hx[4];
    if (j == 3) {
#pragma unroll
      for (int i = 0; i < 4; ++i) hx[i] = __builtin_amdgcn_raw_buffer_load_b128(rs, hoff, i * (8 * FF * 4), 16);
    }
    SBAR();
    u32x4 Bd[8];
    transpose8(FD, Bd);
    transpose8(G0, Bf);
#pragma unroll
    for (int i = 0; i < 8; ++i) { asm volatile("" : "+v"(Bd[i])); asm volatile("" : "+v"(Bf[i])); }
    STAMP(5);
    if (j == 3) {
      if (__any((badd(hx[0]) | badd(hx[1])) | (badd(hx[2]) | badd(hx[3])))) {
#pragma unroll 1
        for (u32 spin = 0; spin <= SPIN_CAP; ++spin) {
          asm volatile("" ::: "memory");
#pragma unroll
          for (int i = 0; i < 4; ++i) hx[i] = __builtin_amdgcn_raw_buffer_load_b128(rs, hoff, i * (8 * FF * 4), 16);
          if (!__any((badd(hx[0]) | badd(hx[1])) | (badd(hx[2]) | badd(hx[3])))) break;
          __builtin_amdgcn_s_sleep(4);
        }
      }
      float gs[4], us[4];
#pragma unroll
      for (int u = 0; u < 4; ++u) { gs[u] = asf(hx[0][u] << 8) + asf(hx[1][u] << 8); us[u] = asf(hx[2][u] << 8) + asf(hx[3][u] << 8); }
      float gt = fold16(fold32(gs[0], gs[2]), fold32(gs[1], gs[3])), up = fold16(fold32(us[0], us[2]), fold32(us[1], us[3]));
      gt = rbf(gt + dpp<XOR8>(gt)); up = rbf(up + dpp<XOR8>(up));
      const float ge = rbf(gt * frcp(1.f + __builtin_amdgcn_exp2f(-2.302208198f * (gt + 0.044715f * gt * gt * gt))));
      const int fl = 4 * (l & 7) + g4;
      const int hi = (l & 7) < 7 ? 8 * (fl / 7) + fl % 7 : 8 * g4 + 7;
      if (!c2) *(u16*)(smem + L_HB + hi * 2) = (l & 7) < 7 ? (u16)f2bf(ge * up) : (u16)0;
      sig(C_H);
    }
    await(C_H, 1);
    {
      const u32x4 ah = *(const u32x4*)(smem + L_HB + g4 * 16);
#pragma unroll
      for (int i = 0; i < 8; ++i) d[i] = mfma32(ah, Bd[i], zero4);
    }
    STAMP(6);
    await(C_O, 4);
    STAMP(7);
    {
      const float* op = (const float*)(smem + L_OP) + (l & 31);
      const f32x4 mm = *(const f32x4*)(smem + L_MW), ww = *(const f32x4*)(smem + L_MW + 16);
      const float M = fmaxf(fmaxf(mm[0], mm[1]), fmaxf(mm[2], mm[3]));
      const float s0 = __builtin_amdgcn_exp2f(mm[0] - M), s1 = __builtin_amdgcn_exp2f(mm[1] - M), s2 = __builtin_amdgcn_exp2f(mm[2] - M), s3 = __builtin_amdgcn_exp2f(mm[3] - M);
      const float num = (s0 * op[0] + s1 * op[32]) + (s2 * op[64] + s3 * op[96]), dnm = (s0 * ww[0] + s1 * ww[1]) + (s2 * ww[2] + s3 * ww[3]);
      *(u16*)(smem + L_OB + (j * 64 + l) * 2) = (u16)f2bf(num * frcp(dnm));
      asm volatile("s_waitcnt lgkmcnt(0)" ::: "memory");
      const u32x4 ao = *(const u32x4*)(smem + L_OB + j * 128 + g4 * 16);
#pragma unroll
      for (int i = 0; i < 8; ++i) d[i] = mfma32(ao, Bf[i], d[i]);
      const u32x4 e = pack_wide(d, g4 & 1, t0, t1);
      if (l < 32)
        __builtin_amdgcn_raw_buffer_store_b128(e, rs, (int)(WS_OUT + (RG * 1024 + 512 * cg + 128 * j + 8 * (l & 15) + 4 * g4) * 4), 0, 16);
    }
    STAMP(8);
  }
  if (b < 64) {
    const u32 t0 = cnt[C_TAG], t1 = cnt[C_TAG + 1];
    const u32 xw = (l < 8) ? *(const u32*)(p.x + 16 * b + 2 * l) : 0u;
    const int off = (int)(WS_OUT + ((16 * w + (l >> 2)) * 1024 + 16 * b + 4 * (l & 3)) * 4);
    u32x4 y;
#pragma unroll 1
    for (u32 spin = 0; spin <= SPIN_CAP; ++spin) {
      asm volatile("" ::: "memory");
      y = __builtin_amdgcn_raw_buffer_load_b128(rs, off, 0, 16);
      const u64 bl = __ballot(((((y[0] ^ t0) | (y[1] ^ t1)) | ((y[2] ^ t0) | (y[3] ^ t1))) >> 24) != 0u);
      if (!bl) break;
      if (__builtin_popcountll(bl) > 32) __builtin_amdgcn_s_sleep(8);
    }
    float r = fold16(fold32(asf(y[0] << 8), asf(y[2] << 8)), fold32(asf(y[1] << 8), asf(y[3] << 8)));
    r += dpp<XOR8>(r); r += dpp<ROR4>(r);
    if ((l & 12) == 0) *(float*)(smem + L_FP + (w * 16 + 4 * (l & 3) + g4) * 4) = r;
    sig(C_FIN);
    if (w == 0) {
      await(C_FIN, 8);
      if (l < 8) {
        const f32x2* fp = (const f32x2*)(smem + L_FP) + l;
        const f32x2 a = ((fp[0] + fp[8]) + (fp[16] + fp[24])) + ((fp[32] + fp[40]) + (fp[48] + fp[56]));
        *(u32*)(p.out + 16 * b + 2 * l) = pkbf(bflo(xw) + a[0], bfhi(xw) + a[1]);
      }
      if (b == 0 && l == 0) __hip_atomic_store((gu64*)(p.ws + WS_EPOCH), ((u64)cnt[C_TAG + 5] << 32) | cnt[C_TAG + 4], RLX_AGENT);
    }
  }
  STAMP(9);
}

extern "C" void kernel_launch(void* const* d_in, const int* in_sizes, int n_in, void* d_out, int out_size, void* d_ws, size_t ws_size,
                              hipStream_t stream) {
  Params p;
  p.x = (const u16*)d_in[0];
  p.K = (const u16*)d_in[1];
  p.V = (const u16*)d_in[2];
  p.wq = (const u16*)d_in[3];
  p.wo = (const u16*)d_in[4];
  p.wg = (const u16*)d_in[6];
  p.wu = (const u16*)d_in[7];
  p.wd = (const u16*)d_in[8];
  p.out = (u16*)d_out;
  p.ws = (unsigned char*)d_ws;
  fused_decode<<<NWG, NT, 0, stream>>>(p);
}
```
